```python
import torch

def _softmax(x, axis=-1):
    return torch.softmax(x, dim=axis)
def _gelu(x):
    return torch.nn.functional.gelu(x, approximate="tanh")
def _silu(x):
    return torch.nn.functional.silu(x)
def _sigmoid(x):
    return torch.sigmoid(x)
def _layernorm(x, axis=-1, eps=1e-5):
    m = x.mean(dim=axis, keepdim=True)
    v = x.var(dim=axis, keepdim=True, unbiased=False)
    return (x - m) / torch.sqrt(v + eps)
def _scatter_add(tgt, idx, src, axis=0):
    return tgt.clone().index_add_(axis, idx, src)
def _segment_sum(src, seg, n):
    out = torch.zeros((n,) + src.shape[1:], dtype=src.dtype)
    return out.index_add_(0, seg, src)
def _pad(x, pw):
    flat = tuple(v for pair in reversed(pw) for v in pair)
    return torch.nn.functional.pad(x, flat)

kernel_name = 'recurrent_scan__gqa_block_cached__swiglu_ffn__decode'

def setup_inputs():

    _w = (lambda a: (lambda t: t.to(torch.bfloat16) if t.is_floating_point() else t)(torch.as_tensor(a)))
    def _A_setup():
        B, Sq, D = 2, 1, 576
        Hq, Hkv, Sk, Dh = 32, 16, 32768, 128
        assert Hq % Hkv == 0, (Hq, Hkv)
        return {
            "x": _w((lambda *s: torch.randn(*s, dtype=torch.bfloat16) * 0.05)(B, Sq, D)),
            "k_cache": _w((lambda *s: torch.randn(*s, dtype=torch.bfloat16) * 1.6)(B, Hkv, Sk, Dh)),
            "v_cache": _w((lambda *s: torch.randn(*s, dtype=torch.bfloat16) * 0.2)(B, Hkv, Sk, Dh)),
            "wq": _w((lambda *s: torch.randn(*s, dtype=torch.bfloat16) * 1.6)(D, Hq * Dh) * (D ** -0.5)),
            "wo": _w((lambda *s: torch.randn(*s, dtype=torch.bfloat16) * 0.2)(Hq * Dh, D) * ((Hq * Dh) ** -0.5)),
        }
    _A_in = _A_setup()

    def _B_setup():
        B, S, D, F = 2, 1, 576, 4608
        return {
            "x": _w((lambda *s: torch.randn(*s, dtype=torch.bfloat16) * 0.05)(B, S, D)),
            "w_gate": _w((lambda *s: torch.randn(*s, dtype=torch.bfloat16) * 0.1)(D, F)),
            "w_up": _w((lambda *s: torch.randn(*s, dtype=torch.bfloat16) * 0.1)(D, F)),
            "w_down": _w((lambda *s: torch.randn(*s, dtype=torch.bfloat16) * 0.1)(F, D)),
        }
    _B_in = _B_setup()

    return {
        "A_x": _A_in["x"],
        "A_k_cache": _A_in["k_cache"],
        "A_v_cache": _A_in["v_cache"],
        "A_wq": _A_in["wq"],
        "A_wo": _A_in["wo"],
        "B_x": _B_in["x"],
        "B_w_gate": _B_in["w_gate"],
        "B_w_up": _B_in["w_up"],
        "B_w_down": _B_in["w_down"],
    }

def reference(A_x, A_k_cache, A_v_cache, A_wq, A_wo, B_x, B_w_gate, B_w_up, B_w_down):
    def _A_ref(x, k_cache, v_cache, wq, wo):
        B, Sq, D = x.shape
        Hq, Dh = 32, 128
        Hkv = k_cache.shape[1]
        rep = Hq // Hkv
        q = (lambda x, *d: x.permute(*d))((x @ wq).reshape(B, Sq, Hq, Dh), 0, 2, 1, 3)
        k = (lambda x, n, axis: torch.repeat_interleave(x, n, dim=axis))(k_cache, rep, axis=1)
        v = (lambda x, n, axis: torch.repeat_interleave(x, n, dim=axis))(v_cache, rep, axis=1)
        scores = torch.einsum("bhqd,bhkd->bhqk", q, k) * (Dh ** -0.5)
        p = _softmax(scores, axis=-1)
        o = torch.einsum("bhqk,bhkd->bhqd", p, v)
        return (lambda x, *d: x.permute(*d))(o, 0, 2, 1, 3).reshape(B, Sq, Hq * Dh) @ wo

    def _B_ref(x, w_gate, w_up, w_down):
        g = _silu(x @ w_gate)
        u = x @ w_up
        return (g * u) @ w_down

    x = A_x
    xn = _layernorm(x, axis=-1)
    s = _A_ref(xn, A_k_cache, A_v_cache, A_wq, A_wo)
    g = _silu(_B_ref(xn, B_w_gate, B_w_up, B_w_down))
    return x + s * g
```

```cpp
#include <hip/hip_runtime.h>
#include <hip/hip_bf16.h>
#include <stdint.h>

#define DIAG 0
#define P_W0 22
#define P_WMIN 16
#define P_WMAX 44
#define P_TUS 71.0
#define P_KUS 4.0
#define P_WTAIL 40

namespace {

#define LAS __attribute__((address_space(3)))
typedef unsigned u32x2 __attribute__((ext_vector_type(2)));
typedef unsigned u32x4 __attribute__((ext_vector_type(4)));
typedef int i32x4 __attribute__((ext_vector_type(4)));
typedef float f32x2 __attribute__((ext_vector_type(2)));
typedef float f32x4 __attribute__((ext_vector_type(4)));
typedef __bf16 bf16x2 __attribute__((ext_vector_type(2)));
typedef __bf16 bf16x4 __attribute__((ext_vector_type(4)));
typedef __bf16 bf16x8 __attribute__((ext_vector_type(8)));
typedef __amdgpu_buffer_rsrc_t rsrc_t;

constexpr int D = 576, FF = 4608, NWG = 256, NTHR = 384;
constexpr int AUX_NT = 2, AUX_SC1 = 16;

constexpr unsigned WS_EPOCH = 0;
constexpr unsigned WS_Q = 4096;
constexpr unsigned WS_ML = WS_Q + 262144;
constexpr unsigned WS_H = WS_ML + 32768;
constexpr unsigned WS_PART = WS_H + 36864;
constexpr unsigned WS_SP = WS_PART + 262144;
constexpr unsigned WS_YP = WS_SP + 147456;
constexpr unsigned WS_END = WS_YP + 110592;
constexpr unsigned WS_DIAG = 1u << 20;

constexpr unsigned L_RING = 0;
constexpr unsigned L_WQP = 131072;
constexpr unsigned P_STRIDE = 8256;
constexpr unsigned L_LANDED = 151552;
constexpr unsigned L_DONE = L_LANDED + 256;
constexpr unsigned L_CNTQ = L_DONE + 16, L_CNTM = L_CNTQ + 4, L_CNTP = L_CNTQ + 8, L_FLAGF = L_CNTQ + 12, L_FLAGH = L_CNTQ + 16;
constexpr unsigned L_MX = L_DONE + 64;
constexpr unsigned L_LS = L_MX + 32;
constexpr unsigned L_F = L_LS + 32;
constexpr unsigned L_Q16 = L_LANDED + 512;
constexpr unsigned L_HB = L_Q16 + 512;
constexpr unsigned HB_STRIDE = 832;
constexpr unsigned L_XN = L_HB + 1792;
constexpr unsigned L_RED = L_XN + 4608;
constexpr unsigned L_XB = L_RED;
constexpr unsigned XB_STRIDE = 1216;
constexpr unsigned L_END = L_RED + 4096;
static_assert(L_END <= 163840, "LDS");
constexpr int NQP = 20;

struct Args {
  const void* x; const void* k; const void* v; const void* wq; const void* wo;
  const void* wg; const void* wu; const void* wd; void* out; void* ws;
};

__device__ __forceinline__ float bf_lo(unsigned u) { return __builtin_bit_cast(float, u << 16); }
__device__ __forceinline__ float bf_hi(unsigned u) { return __builtin_bit_cast(float, u & 0xffff0000u); }
__device__ __forceinline__ unsigned pk_bf16(float lo, float hi) {
  f32x2 v = {lo, hi};
  return __builtin_bit_cast(unsigned, __builtin_convertvector(v, bf16x2));
}
__device__ __forceinline__ float rbf(float x) { return bf_lo(pk_bf16(x, 0.f)); }
__device__ __forceinline__ float silu_f(float x) { return x * __builtin_amdgcn_rcpf(1.f + __builtin_amdgcn_exp2f(-1.4426950408889634f * x)); }
template <int CTRL> __device__ __forceinline__ float dpp(float x) {
  return __builtin_bit_cast(float, __builtin_amdgcn_mov_dpp(__builtin_bit_cast(int, x), CTRL, 0xf, 0xf, true));
}
constexpr int DPP_XOR1 = 0xB1, DPP_XOR2 = 0x4E, DPP_ROR4 = 0x124, DPP_ROR8 = 0x128;
__device__ __forceinline__ float x16_sum(float x) {
  auto s = __builtin_amdgcn_permlane16_swap(__float_as_uint(x), __float_as_uint(x), false, false);
  return __uint_as_float(s[0]) + __uint_as_float(s[1]);
}
__device__ __forceinline__ float x32_sum(float x) {
  auto t = __builtin_amdgcn_permlane32_swap(__float_as_uint(x), __float_as_uint(x), false, false);
  return __uint_as_float(t[0]) + __uint_as_float(t[1]);
}
__device__ __forceinline__ float x16_max(float x) {
  auto s = __builtin_amdgcn_permlane16_swap(__float_as_uint(x), __float_as_uint(x), false, false);
  return fmaxf(__uint_as_float(s[0]), __uint_as_float(s[1]));
}
__device__ __forceinline__ float x32_max(float x) {
  auto t = __builtin_amdgcn_permlane32_swap(__float_as_uint(x), __float_as_uint(x), false, false);
  return fmaxf(__uint_as_float(t[0]), __uint_as_float(t[1]));
}
__device__ __forceinline__ float half_sum(float v) {
  v += dpp<DPP_XOR1>(v); v += dpp<DPP_XOR2>(v); v += dpp<DPP_ROR4>(v); v += dpp<DPP_ROR8>(v);
  return x16_sum(v);
}
__device__ __forceinline__ float par_sum(float v) {
  v += dpp<DPP_XOR2>(v); v += dpp<DPP_ROR4>(v); v += dpp<DPP_ROR8>(v);
  return x32_sum(x16_sum(v));
}
__device__ __forceinline__ float par_max(float v) {
  v = fmaxf(v, dpp<DPP_XOR2>(v)); v = fmaxf(v, dpp<DPP_ROR4>(v)); v = fmaxf(v, dpp<DPP_ROR8>(v));
  return x32_max(x16_max(v));
}
__device__ __forceinline__ f32x4 mfma(bf16x8 a, bf16x8 b, f32x4 c) { return __builtin_amdgcn_mfma_f32_16x16x32_bf16(a, b, c, 0, 0, 0); }
__device__ __forceinline__ bf16x8 as_frag(u32x4 v) { return __builtin_bit_cast(bf16x8, v); }
__device__ __forceinline__ rsrc_t mk_rsrc(const void* p, unsigned bytes) { return __builtin_amdgcn_make_buffer_rsrc((void*)p, 0, (int)bytes, 0x00020000); }
#define CFENCE() asm volatile("" ::: "memory")
#define LGKM0() asm volatile("s_waitcnt lgkmcnt(0)" ::: "memory")
#define BARRIER() do { __builtin_amdgcn_sched_barrier(0); asm volatile("s_barrier" ::: "memory"); __builtin_amdgcn_sched_barrier(0); } while (0)

typedef LAS unsigned char* lds_t;
__device__ __forceinline__ bf16x8 lds_frag(lds_t L, unsigned off) { return *(LAS bf16x8*)(L + off); }
__device__ __forceinline__ bf16x8 lds_tr2(lds_t L, unsigned off0, unsigned off1) {
  bf16x4 a = __builtin_amdgcn_ds_read_tr16_b64_v4bf16((LAS bf16x4*)(L + off0));
  bf16x4 b = __builtin_amdgcn_ds_read_tr16_b64_v4bf16((LAS bf16x4*)(L + off1));
  return __builtin_shufflevector(a, b, 0, 1, 2, 3, 4, 5, 6, 7);
}
template <int SLEEP> __device__ __forceinline__ int lds_wait_ge(lds_t L, unsigned off, int need, int& budget) {
  int v; unsigned tmp;
  budget = __builtin_amdgcn_readfirstlane(budget); need = __builtin_amdgcn_readfirstlane(need);
  asm volatile("1:\n\tds_read_b32 %1, %3\n\ts_waitcnt lgkmcnt(0)\n\tv_readfirstlane_b32 %0, %1\n\ts_cmp_ge_i32 %0, %4\n\ts_cbranch_scc1 2f\n\t"
               "s_sub_i32 %2, %2, 1\n\ts_sleep %5\n\ts_cmp_gt_i32 %2, 0\n\ts_cbranch_scc1 1b\n2:"
               : "=&s"(v), "=&v"(tmp), "+s"(budget) : "v"((unsigned)(uintptr_t)L + off), "s"(need), "i"(SLEEP) : "scc", "memory");
  return v;
}
__device__ __forceinline__ void lds_arrive(lds_t L, unsigned off) {
  asm volatile("s_mov_b64 exec, 1\n\tds_add_u32 %0, %1\n\ts_mov_b64 exec, -1" :: "v"((unsigned)(uintptr_t)L + off), "v"(1u) : "memory");
}
#define LDS_PUT1 "s_mov_b64 exec, 1\n\tds_write_b32 %[pa], %[pv]\n\ts_mov_b64 exec, -1"
constexpr unsigned OOB = 0x40000000u;

struct LnStat { float m, s; };
__device__ __forceinline__ LnStat ln_stats(const unsigned (&xr)[9]) {
  float s = 0.f;
#pragma unroll
  for (int j = 0; j < 9; ++j) s += bf_lo(xr[j]) + bf_hi(xr[j]);
  const float mean = half_sum(s) * (1.f / D);
  float q = 0.f;
#pragma unroll
  for (int j = 0; j < 9; ++j) { const float a = bf_lo(xr[j]) - mean, b = bf_hi(xr[j]) - mean; q = fmaf(a, a, q); q = fmaf(b, b, q); }
  const float var = half_sum(q) * (1.f / D);
  const float m = rbf(mean), sd = __builtin_amdgcn_rcpf(rbf(__builtin_amdgcn_sqrtf(rbf(rbf(var) + 1e-5f))));
  LnStat r; r.m = m; r.s = sd;
  return r;
}
__device__ __forceinline__ unsigned ln_pair(unsigned u, float m, float sd) { const unsigned d = pk_bf16(bf_lo(u) - m, bf_hi(u) - m); return pk_bf16(bf_lo(d) * sd, bf_hi(d) * sd); }

#define DMA4(V0, V1, V2, V3, SRD, SOFF) \
  "buffer_load_dwordx4 " V0 ", " SRD ", " SOFF " offen nt lds\n\t" \
  "buffer_load_dwordx4 " V1 ", " SRD ", " SOFF " offen offset:1024 nt lds\n\t" \
  "buffer_load_dwordx4 " V2 ", " SRD ", " SOFF " offen offset:2048 nt lds\n\t" \
  "buffer_load_dwordx4 " V3 ", " SRD ", " SOFF " offen offset:3072 nt lds\n\t"
__device__ __forceinline__ void dma4(i32x4 srd, unsigned v0, unsigned v1, unsigned v2, unsigned v3, unsigned soff, unsigned ldsaddr) {
  asm volatile("s_mov_b32 m0, %6\n\ts_nop 0\n\t" DMA4("%0", "%1", "%2", "%3", "%4", "%5")
               :: "v"(v0), "v"(v1), "v"(v2), "v"(v3), "s"(srd), "s"(soff), "s"(ldsaddr) : "memory");
}
#define WV1(n) "s_waitcnt vmcnt(" #n ")\n\ts_setpc_b64 s[40:41]\n\t"
#define GRP_LOADS DMA4("%[v0]", "%[v1]", "%[v2]", "%[v3]", "%[srd]", "%[off]")
#define GRP_END "v_add_u32 v40, 4, v40\n\ts_add_u32 m0, m0, 0x1000\n\ts_add_u32 %[off], %[off], %[step]\n\t" \
                "s_swappc_b64 s[40:41], s[42:43]\n\tds_write_b32 %[la], v40\n\t"
__device__ __forceinline__ void stream_slots(i32x4 srd, unsigned v0, unsigned v1, unsigned v2, unsigned v3, unsigned off, unsigned step,
                                             int& slot, int send, int& issued, int& w4, int& sched, int& freeto, int& budget,
                                             int w0, int kg, int drain, unsigned ring, unsigned la, unsigned da) {
  constexpr int TAU = (int)(P_TUS * 100.0 / 128.0 * 256.0 + 0.5);
  asm volatile(
      "s_getpc_b64 s[44:45]\n\t"
      "s_branch 3f\n\t"
      WV1(0) WV1(1) WV1(2) WV1(3) WV1(4) WV1(5) WV1(6) WV1(7) WV1(8) WV1(9) WV1(10) WV1(11) WV1(12) WV1(13) WV1(14) WV1(15)
      WV1(16) WV1(17) WV1(18) WV1(19) WV1(20) WV1(21) WV1(22) WV1(23) WV1(24) WV1(25) WV1(26) WV1(27) WV1(28) WV1(29) WV1(30) WV1(31)
      WV1(32) WV1(33) WV1(34) WV1(35) WV1(36) WV1(37) WV1(38) WV1(39) WV1(40) WV1(41) WV1(42) WV1(43) WV1(44) WV1(45) WV1(46) WV1(47)
      "5:\n\t"
      "s_waitcnt vmcnt(0)\n\t"
      "v_mov_b32 v40, %[iss]\n\t"
      "ds_write_b32 %[la], v40\n"
      "6:\n\t"
      "ds_read_b128 v[44:47], %[da]\n\t"
      "s_waitcnt lgkmcnt(0)\n\t"
      "v_min3_i32 v44, v44, v45, v46\n\t"
      "v_min_i32 v44, v44, v47\n\t"
      "s_sub_i32 %[bud], %[bud], 1\n\t"
      "v_readfirstlane_b32 %[fr], v44\n\t"
      "s_cmp_ge_i32 %[fr], s48\n\t"
      "s_cbranch_scc1 2f\n\t"
      "s_sleep 1\n\t"
      "s_cmp_gt_i32 %[bud], 0\n\t"
      "s_cbranch_scc1 6b\n\t"
      "s_branch 2f\n"
      "3:\n\t"
      "s_add_u32 s44, s44, 4\n\t"
      "s_addc_u32 s45, s45, 0\n\t"
      "s_cmp_ge_i32 %[slot], %[send]\n\t"
      "s_cbranch_scc1 4f\n"
      "1:\n\t"
      "s_sub_i32 s48, %[slot], 7\n\t"
      "s_cmp_ge_i32 %[fr], s48\n\t"
      "s_cbranch_scc0 5b\n"
      "2:\n\t"
      "s_and_b32 s48, %[slot], 7\n\t"
      "s_lshl_b32 s48, s48, 14\n\t"
      "s_add_u32 m0, s48, %[ring]\n\t"
      "s_sub_i32 s48, %[iss], %[w4]\n\t"
      GRP_LOADS
      "ds_read_b128 v[44:47], %[da]\n\t"
      "s_memrealtime s[46:47]\n\t"
      "v_mov_b32 v40, s48\n\t"
      "s_lshl_b32 s48, %[w4], 3\n\t"
      "s_add_u32 s42, s44, s48\n\t"
      "s_addc_u32 s43, s45, 0\n\t"
      GRP_END
      GRP_LOADS
      "s_waitcnt lgkmcnt(0)\n\t"
      "v_min3_i32 v44, v44, v45, v46\n\t"
      "v_min_i32 v44, v44, v47\n\t"
      "s_lshl_b32 s46, s46, 8\n\t"
      "v_readfirstlane_b32 %[fr], v44\n\t"
      "s_sub_i32 s46, s46, %[sch]\n\t"
      "s_mul_i32 s46, s46, %[kg]\n\t"
      "s_ashr_i32 s46, s46, 16\n\t"
      "s_add_i32 s46, s46, %[w0]\n\t"
      "s_max_i32 s46, s46, %[wmin]\n\t"
      "s_min_i32 s46, s46, %[wmax]\n\t"
      GRP_END
      GRP_LOADS
      "s_add_i32 %[sch], %[sch], %[tau]\n\t"
      "s_add_i32 %[iss], %[iss], 16\n\t"
      "s_add_i32 %[slot], %[slot], 1\n\t"
      GRP_END
      GRP_LOADS
      "s_mov_b32 %[w4], s46\n\t"
      GRP_END
      "s_cmp_lt_i32 %[slot], %[send]\n\t"
      "s_cbranch_scc1 1b\n"
      "4:\n\t"
      "s_cmp_lg_u32 %[drain], 2\n\t"
      "s_cbranch_scc1 8f\n\t"
      "s_mov_b32 s48, %[wmax4]\n"
      "7:\n\t"
      "s_lshl_b32 s46, s48, 3\n\t"
      "s_add_u32 s42, s44, s46\n\t"
      "s_addc_u32 s43, s45, 0\n\t"
      "s_sub_i32 s46, %[iss], s48\n\t"
      "v_mov_b32 v40, s46\n\t"
      "s_swappc_b64 s[40:41], s[42:43]\n\t"
      "ds_write_b32 %[la], v40\n\t"
      "s_sub_i32 s48, s48, 4\n\t"
      "s_cmp_ge_i32 s48, 0\n\t"
      "s_cbranch_scc1 7b\n"
      "8:"
      : [off] "+s"(off), [slot] "+s"(slot), [iss] "+s"(issued), [w4] "+s"(w4), [sch] "+s"(sched), [fr] "+s"(freeto), [bud] "+s"(budget)
      : [srd] "s"(srd), [v0] "v"(v0), [v1] "v"(v1), [v2] "v"(v2), [v3] "v"(v3), [step] "s"(step), [send] "s"(send), [w0] "s"(w0), [kg] "s"(kg),
        [drain] "s"(drain), [ring] "s"(ring), [la] "v"(la), [da] "v"(da), [tau] "i"(TAU), [wmin] "i"(P_WMIN - 4), [wmax] "i"(P_WMAX - 4),
        [wmax4] "i"((P_WTAIL - 8) & ~3)
      : "s40", "s41", "s42", "s43", "s44", "s45", "s46", "s47", "s48", "v40", "v44", "v45", "v46", "v47", "scc", "memory");
}
__device__ __forceinline__ i32x4 mk_srd(const void* p, unsigned bytes) {
  const unsigned long long a = (unsigned long long)p;
  i32x4 r = {(int)(unsigned)a, (int)((unsigned)(a >> 32) & 0xffffu), (int)bytes, 0x00020000};
  return r;
}

#define STAMP(i) do {} while (0)

__global__ void __launch_bounds__(NTHR, 2) fused_decode(Args a) {
  __shared__ __attribute__((aligned(16))) unsigned char lds_raw[163840];
  lds_t L = (lds_t)lds_raw;
  const int lane = threadIdx.x & 63, wave = __builtin_amdgcn_readfirstlane(threadIdx.x >> 6);
  const int wg = blockIdx.x;
  const int kvh = (wg >> 3) & 15, bat = wg >> 7, r16 = bat * 8 + (wg & 7);
  const int rb = r16 >> 2, cbq = r16 & 3;
  const int qrow0 = rb < 2 ? 160 * rb : 64 + 128 * rb, nks = rb < 2 ? 5 : 4;
  const int role = (r16 - kvh) & 15;
  const int didx = kvh * 7 + role - 9;
  const bool is_wo = role < 9, is_wd = !is_wo && didx < 108;
  const int rbd = didx / 9, cbd = didx - rbd * 9;
  int budget = 1 << 22;

  if (wave == 0) {
    __builtin_amdgcn_s_setprio(3);
    const unsigned t0 = (unsigned)__builtin_amdgcn_s_memrealtime();
    const unsigned lb = (unsigned)(uintptr_t)L;
    const unsigned wswz = ((unsigned)(lane & 7) ^ (((unsigned)(lane >> 4) & 3u) << 1)) * 16u;
    {
      const i32x4 srd = mk_srd(a.wq, D * 4096 * 2);
      const unsigned voff = (unsigned)(lane >> 3) * 8192u + wswz;
      unsigned soff = (unsigned)qrow0 * 8192u + (unsigned)(kvh * 256 + cbq * 64) * 2u;
#pragma unroll 1
      for (int p = 0; p < nks; ++p) { dma4(srd, voff, voff + 64512u, voff + 129024u, voff + 193536u, soff, lb + L_WQP + p * 4096); soff += 262144u; }
    }
    *(volatile LAS int*)(L + L_LANDED + lane * 4) = 0;
    *(volatile LAS int*)(L + L_DONE + lane * 4) = lane < 4 ? lane : 0;
    LGKM0();
    BARRIER();
    const unsigned t = (unsigned)(lane >> 4), cc = (unsigned)(lane & 15);
    constexpr int KG = (int)(P_KUS / 100.0 * 256.0 + 0.5);
    int slot = 0, issued = NQP, w4 = P_W0 - 4, sched = (int)(t0 * 256u), freeto = 0;
#pragma unroll 1
    for (int ph = 0; ph < 3; ++ph) {
      unsigned v0, v1, v2, v3, off = 0, step = 4096;
      int send = 64 * (ph + 1), w0 = P_W0 - 4, kg = KG;
      i32x4 srd;
      if (ph == 0) {
        srd = mk_srd((const char*)a.k + (size_t)wg * 1048576u, 1048576u);
        v0 = t * 256u + ((cc ^ t) * 16u); v1 = t * 256u + ((cc ^ (4u + t)) * 16u);
        v2 = t * 256u + ((cc ^ (8u + t)) * 16u); v3 = t * 256u + ((cc ^ (12u + t)) * 16u);
      } else if (ph == 1) {
        srd = mk_srd((const char*)a.v + (size_t)wg * 1048576u, 1048576u);
        v0 = v1 = t * 256u + ((cc ^ (t << 1)) * 16u);
        v2 = v3 = t * 256u + ((cc ^ ((t | 4u) << 1)) * 16u);
      } else {
        srd = is_wo ? mk_srd(a.wo, 4096 * D * 2) : mk_srd(a.wd, FF * D * 2);
        v0 = (unsigned)(lane >> 3) * 1152u + wswz; v1 = v0 + 8192u; v2 = v0 + 16384u; v3 = v0 + 24576u;
        off = is_wo ? (unsigned)(kvh * 256 * 1152 + role * 128) : (unsigned)(rbd * 384 * 1152 + cbd * 128);
        step = 36864u; send = 128 + (is_wo ? 2 : is_wd ? 3 : 0); w4 = w0 = P_WTAIL - 4; kg = 0;
      }
      stream_slots(srd, v0, v1, v2, v3, off, step, slot, send, issued, w4, sched, freeto, budget, w0, kg, ph, lb + L_RING,
                   lb + L_LANDED + lane * 4, lb + L_DONE);
      if (ph < 2) STAMP(1 + ph);
    }
    return;
  }

  const rsrc_t rws = mk_rsrc(a.ws, WS_END);
  const rsrc_t rx = mk_rsrc(a.x, 2 * D * 2);
  const unsigned epoch = __builtin_amdgcn_raw_buffer_load_b32(rws, 0, WS_EPOCH, AUX_SC1);
  unsigned xr[9];
#pragma unroll
  for (int j = 0; j < 9; ++j) xr[j] = __builtin_amdgcn_raw_buffer_load_b32(rx, (lane >> 5) * 1152 + (lane & 31) * 4 + j * 128, 0, 0);
  const int n16 = lane & 15, g4 = lane >> 4;

  if (wave == 5) {
    BARRIER();
    const unsigned tag = __builtin_amdgcn_readfirstlane(epoch) * 1664525u + 1013904223u;
    if (wg < 72) {
      const LnStat st = ln_stats(xr);
#pragma unroll
      for (int j = 0; j < 9; ++j) {
        const unsigned u = ln_pair(xr[j], st.m, st.s);
        const unsigned o = L_XN + (unsigned)(((lane & 31) + 32 * j) * 16 + (lane >> 5) * 4);
        *(LAS float*)(L + o) = bf_lo(u); *(LAS float*)(L + o + 8) = bf_hi(u);
      }
      LGKM0();
      const rsrc_t rg = mk_rsrc(a.wg, D * FF * 2), ru = mk_rsrc(a.wu, D * FF * 2);
      const unsigned voff = (unsigned)(lane >> 3) * 9216u + (unsigned)(lane & 7) * 16u + (unsigned)wg * 128u;
      float acc[32];
#pragma unroll
      for (int i = 0; i < 32; ++i) acc[i] = 0.f;
      u32x4 ga = __builtin_amdgcn_raw_buffer_load_b128(rg, voff, 0, AUX_NT), ua = __builtin_amdgcn_raw_buffer_load_b128(ru, voff, 0, AUX_NT);
      u32x4 gb = __builtin_amdgcn_raw_buffer_load_b128(rg, voff, 73728, AUX_NT), ub = __builtin_amdgcn_raw_buffer_load_b128(ru, voff, 73728, AUX_NT);
#define GU_STEP(G, U, it) do { \
        const f32x2 xv = *(LAS f32x2*)(L + L_XN + (unsigned)((it) * 8 + (lane >> 3)) * 8u); \
        _Pragma("unroll") for (int e = 0; e < 4; ++e) { \
          const float g0 = bf_lo(G[e]), g1 = bf_hi(G[e]), u0 = bf_lo(U[e]), u1 = bf_hi(U[e]); \
          acc[4 * e + 0] = fmaf(g0, xv.x, acc[4 * e + 0]); acc[4 * e + 1] = fmaf(g1, xv.x, acc[4 * e + 1]); \
          acc[4 * e + 2] = fmaf(u0, xv.x, acc[4 * e + 2]); acc[4 * e + 3] = fmaf(u1, xv.x, acc[4 * e + 3]); \
          acc[16 + 4 * e + 0] = fmaf(g0, xv.y, acc[16 + 4 * e + 0]); acc[16 + 4 * e + 1] = fmaf(g1, xv.y, acc[16 + 4 * e + 1]); \
          acc[16 + 4 * e + 2] = fmaf(u0, xv.y, acc[16 + 4 * e + 2]); acc[16 + 4 * e + 3] = fmaf(u1, xv.y, acc[16 + 4 * e + 3]); } \
        __builtin_amdgcn_sched_barrier(0); \
        G = __builtin_amdgcn_raw_buffer_load_b128(rg, voff, ((it) + 2) * 73728, AUX_NT); \
        U = __builtin_amdgcn_raw_buffer_load_b128(ru, voff, ((it) + 2) * 73728, AUX_NT); \
        __builtin_amdgcn_sched_barrier(0); } while (0)
#pragma unroll 1
      for (int it = 0; it < 72; it += 2) { GU_STEP(ga, ua, it); GU_STEP(gb, ub, it + 1); }
#undef GU_STEP
      float r16v[16], r8[8], r4[4];
#pragma unroll
      for (int i = 0; i < 16; ++i) { const bool hi = lane & 32; r16v[i] = (hi ? acc[16 + i] : acc[i]) + __shfl_xor(hi ? acc[i] : acc[16 + i], 32); }
#pragma unroll
      for (int i = 0; i < 8; ++i) { const bool hi = lane & 16; r8[i] = (hi ? r16v[8 + i] : r16v[i]) + __shfl_xor(hi ? r16v[i] : r16v[8 + i], 16); }
#pragma unroll
      for (int i = 0; i < 4; ++i) { const bool hi = lane & 8; r4[i] = (hi ? r8[4 + i] : r8[i]) + __shfl_xor(hi ? r8[i] : r8[4 + i], 8); }
      const float h0 = rbf(silu_f(rbf(r4[0]))) * rbf(r4[2]), h1 = rbf(silu_f(rbf(r4[1]))) * rbf(r4[3]);
      const unsigned pair = (unsigned)(wg * 32 + (lane & 7) * 4 + ((lane >> 4) & 1) * 2 + ((lane >> 3) & 1));
      const u32x2 gr = {pk_bf16(h0, h1), tag};
      __builtin_amdgcn_raw_buffer_store_b64(gr, rws, ((unsigned)(lane >> 5) * 2304u + pair) * 8u, WS_H, AUX_SC1);
      STAMP(1);
    }
    lds_wait_ge<32>(L, L_CNTP, 4, budget);
    {
      const float mw = fmaxf(fmaxf(*(LAS float*)(L + L_MX + (lane & 1) * 4), *(LAS float*)(L + L_MX + 8 + (lane & 1) * 4)),
                             fmaxf(*(LAS float*)(L + L_MX + 16 + (lane & 1) * 4), *(LAS float*)(L + L_MX + 24 + (lane & 1) * 4)));
      const float lw = (*(LAS float*)(L + L_LS + (lane & 1) * 4) + *(LAS float*)(L + L_LS + 8 + (lane & 1) * 4)) +
                       (*(LAS float*)(L + L_LS + 16 + (lane & 1) * 4) + *(LAS float*)(L + L_LS + 24 + (lane & 1) * 4));
      {
        const u32x4 gr = {__float_as_uint(mw), tag, __float_as_uint(lw), tag};
        __builtin_amdgcn_raw_buffer_store_b128(gr, rws, lane < 2 ? (unsigned)(wg * 128 + lane * 16) : OOB, WS_ML, AUX_SC1);
      }
      u32x4 ml;
#pragma unroll 1
      for (;;) {
        CFENCE();
        ml = __builtin_amdgcn_raw_buffer_load_b128(rws, (unsigned)(((wg & ~7) + ((lane >> 1) & 7)) * 128 + (lane & 1) * 16), WS_ML, AUX_SC1);
        if (__all((ml.y == tag) & (ml.w == tag)) || (budget -= 64) < 0) break;
        __builtin_amdgcn_s_sleep(16);
      }
      const float ms = __uint_as_float(ml.x);
      float M = fmaxf(ms, dpp<DPP_XOR2>(ms)); M = fmaxf(M, dpp<DPP_ROR4>(M)); M = fmaxf(M, dpp<DPP_ROR8>(M));
      float Ls = __uint_as_float(ml.z) * __builtin_amdgcn_exp2f(ms - M);
      Ls += dpp<DPP_XOR2>(Ls); Ls += dpp<DPP_ROR4>(Ls); Ls += dpp<DPP_ROR8>(Ls);
      *(LAS float*)(L + L_F + (lane & 1) * 4) = __builtin_amdgcn_exp2f(mw - M) * __builtin_amdgcn_rcpf(Ls);
      lds_arrive(L, L_FLAGF);
      STAMP(2);
    }
    if (is_wd) {
      u32x4 hv[3];
#pragma unroll 1
      for (;;) {
        CFENCE();
        bool ok = true;
#pragma unroll
        for (int j = 0; j < 3; ++j) {
          hv[j] = __builtin_amdgcn_raw_buffer_load_b128(rws, (unsigned)((lane >> 5) * 2304 + rbd * 192) * 8u + (unsigned)((lane & 31) + 32 * j) * 16u, WS_H, AUX_SC1);
          ok = ok & (hv[j].y == tag) & (hv[j].w == tag);
        }
        if (__all(ok) || (budget -= 64) < 0) break;
        __builtin_amdgcn_s_sleep(32);
      }
#pragma unroll
      for (int j = 0; j < 3; ++j) { const u32x2 t2 = {hv[j].x, hv[j].z}; *(LAS u32x2*)(L + L_HB + (lane >> 5) * HB_STRIDE + ((lane & 31) + 32 * j) * 8) = t2; }
      lds_arrive(L, L_FLAGH);
      STAMP(3);
    }
    return;
  }

  const int c = wave - 1;
  const int tq = (lane >> 2) & 3, tp = lane & 3;
  BARRIER();
  STAMP(0);
  const unsigned tag = __builtin_amdgcn_readfirstlane(epoch) * 1664525u + 1013904223u;
  const unsigned wt0 = (unsigned)(8 * g4 + tq) * 128u + (unsigned)(((2 * c + (tp >> 1)) ^ ((tq >> 1) << 1)) * 16 + (tp & 1) * 8);
  const unsigned wt1 = (unsigned)(8 * g4 + 4 + tq) * 128u + (unsigned)(((2 * c + (tp >> 1)) ^ ((2 + (tq >> 1)) << 1)) * 16 + (tp & 1) * 8);
  const unsigned done_addr = (unsigned)(uintptr_t)L + L_DONE + (unsigned)c * 4u;
  int landed;
  bf16x8 qf[4];
  {
    const LnStat st = ln_stats(xr);
#pragma unroll
    for (int j = 0; j < 9; ++j)
      *(LAS unsigned*)(L + L_XB + (lane >> 5) * XB_STRIDE + ((lane & 31) + 32 * j) * 4) = ln_pair(xr[j], st.m, st.s);
    bf16x8 xa[5];
#pragma unroll
    for (int ks = 0; ks < 5; ++ks) xa[ks] = lds_frag(L, L_XB + (lane & 1) * XB_STRIDE + qrow0 * 2 + ks * 64 + g4 * 16);
    landed = lds_wait_ge<1>(L, L_LANDED, NQP, budget);
    f32x4 d = {0.f, 0.f, 0.f, 0.f};
#pragma unroll
    for (int ks = 0; ks < 4; ++ks) d = mfma(xa[ks], lds_tr2(L, L_WQP + ks * 4096 + wt0, L_WQP + ks * 4096 + wt1), d);
    if (nks == 5) d = mfma(xa[4], lds_tr2(L, L_WQP + 16384 + wt0, L_WQP + 16384 + wt1), d);
    {
      const u32x2 gr = {__float_as_uint(g4 ? d[1] : d[0]), tag};
      __builtin_amdgcn_raw_buffer_store_b64(gr, rws, lane < 32 ? (unsigned)(g4 * 1024 + n16) * 8u : OOB, WS_Q + (unsigned)(kvh * 2048 + rb * 256 + cbq * 64 + c * 16) * 8u, AUX_SC1);
    }
    STAMP(1);
    u32x2 qp[4];
#pragma unroll 1
    for (;;) {
      CFENCE();
      bool ok = true;
#pragma unroll
      for (int j = 0; j < 4; ++j) {
        qp[j] = __builtin_amdgcn_raw_buffer_load_b64(rws, (unsigned)lane * 8u + j * 2048, WS_Q + (unsigned)(kvh * 2048 + bat * 1024 + c * 64) * 8u, AUX_SC1);
        ok = ok & (qp[j].y == tag);
      }
      if (__all(ok) || (budget -= 64) < 0) break;
      __builtin_amdgcn_s_sleep(2);
    }
    const float qv = (__uint_as_float(qp[0].x) + __uint_as_float(qp[1].x)) + (__uint_as_float(qp[2].x) + __uint_as_float(qp[3].x));
    *(LAS unsigned short*)(L + L_Q16 + (c * 64 + lane) * 2) = (unsigned short)pk_bf16(qv, 0.f);
    lds_arrive(L, L_CNTQ);
    lds_wait_ge<1>(L, L_CNTQ, 4, budget);
#pragma unroll
    for (int kk = 0; kk < 4; ++kk) qf[kk] = lds_frag(L, L_Q16 + (lane & 1) * 256 + kk * 64 + g4 * 16);
    STAMP(2);
  }
  f32x4 sc[8];
#pragma unroll
  for (int i = 0; i < 8; ++i) sc[i] = f32x4{0.f, 0.f, 0.f, 0.f};
  {
    unsigned ka[4];
#pragma unroll
    for (int kk = 0; kk < 4; ++kk) ka[kk] = L_RING + (unsigned)c * 16384u + (unsigned)n16 * 256u + (unsigned)(((4 * kk + g4) ^ n16) * 16);
#pragma unroll 1
    for (int it = 0; it < 8; ++it) {
      f32x4 cur = {0.f, 0.f, 0.f, 0.f};
#pragma unroll
      for (int hf = 0; hf < 2; ++hf) {
        const int n = 8 * it + 4 * hf + c;
        if (landed < NQP + 16 * (n + 1)) landed = lds_wait_ge<2>(L, L_LANDED, NQP + 16 * (n + 1), budget);
        bf16x8 kf[16];
#pragma unroll
        for (int blk = 0; blk < 4; ++blk)
#pragma unroll
          for (int kk = 0; kk < 4; ++kk) kf[blk * 4 + kk] = lds_frag(L, ka[kk] + hf * 65536 + blk * 4096);
        asm volatile(LDS_PUT1 : "+v"(kf[0]), "+v"(kf[1]), "+v"(kf[2]), "+v"(kf[3]), "+v"(kf[4]), "+v"(kf[5]), "+v"(kf[6]), "+v"(kf[7]),
                                "+v"(kf[8]), "+v"(kf[9]), "+v"(kf[10]), "+v"(kf[11]), "+v"(kf[12]), "+v"(kf[13]), "+v"(kf[14]), "+v"(kf[15])
                     : [pa] "v"(done_addr), [pv] "v"(min(n + 4, 64)) : "memory");
#pragma unroll
        for (int blk = 0; blk < 4; ++blk) {
          f32x4 d = {0.f, 0.f, 0.f, 0.f};
#pragma unroll
          for (int kk = 0; kk < 4; ++kk) d = mfma(kf[blk * 4 + kk], qf[kk], d);
          const bool sel = (n16 >> 1) == 4 * hf + blk;
#pragma unroll
          for (int e = 0; e < 4; ++e) cur[e] = sel ? d[e] : cur[e];
        }
      }
#pragma unroll
      for (int i = 0; i < 7; ++i) sc[i] = sc[i + 1];
      sc[7] = cur;
    }
  }
  STAMP(3);
  {
    constexpr float C2 = 0.08838834764831845f * 1.4426950408889634f;
    float mx = -INFINITY;
#pragma unroll
    for (int i = 0; i < 8; ++i)
#pragma unroll
      for (int e = 0; e < 4; ++e) { sc[i][e] *= C2; mx = fmaxf(mx, sc[i][e]); }
    mx = par_max(mx);
    *(LAS float*)(L + L_MX + c * 8 + (lane & 1) * 4) = mx;
    lds_arrive(L, L_CNTM);
    lds_wait_ge<1>(L, L_CNTM, 4, budget);
    mx = fmaxf(fmaxf(*(LAS float*)(L + L_MX + (lane & 1) * 4), *(LAS float*)(L + L_MX + 8 + (lane & 1) * 4)),
               fmaxf(*(LAS float*)(L + L_MX + 16 + (lane & 1) * 4), *(LAS float*)(L + L_MX + 24 + (lane & 1) * 4)));
    float ls = 0.f;
    const int u = n16 >> 1;
    const unsigned pw = L_WQP + (unsigned)(lane & 1) * P_STRIDE + (unsigned)(64 * (4 * (u >> 2) + c) + 16 * (u & 3) + 4 * g4) * 2u;
#pragma unroll
    for (int i = 0; i < 8; ++i) {
      const float p0 = __builtin_amdgcn_exp2f(sc[i][0] - mx), p1 = __builtin_amdgcn_exp2f(sc[i][1] - mx);
      const float p2 = __builtin_amdgcn_exp2f(sc[i][2] - mx), p3 = __builtin_amdgcn_exp2f(sc[i][3] - mx);
      ls += (p0 + p1) + (p2 + p3);
      const u32x2 pk = {pk_bf16(p0, p1), pk_bf16(p2, p3)};
      *(LAS u32x2*)(L + pw + i * 1024) = pk;
    }
    ls = par_sum(ls);
    *(LAS float*)(L + L_LS + c * 8 + (lane & 1) * 4) = ls;
    lds_arrive(L, L_CNTP);
    lds_wait_ge<1>(L, L_CNTP, 4, budget);
  }
  STAMP(4);
  f32x4 o0 = {0.f, 0.f, 0.f, 0.f}, o1 = {0.f, 0.f, 0.f, 0.f};
  {
    const unsigned vs = (unsigned)((tq | ((g4 & 1) << 2)) << 1);
    const unsigned va0 = L_RING + (unsigned)(8 * g4 + tq) * 256u + (((unsigned)(4 * c + (tp >> 1)) ^ vs) * 16u) + (unsigned)(tp & 1) * 8u;
    const unsigned va1 = L_RING + (unsigned)(8 * g4 + tq) * 256u + (((unsigned)(4 * c + 2 + (tp >> 1)) ^ vs) * 16u) + (unsigned)(tp & 1) * 8u;
    const unsigned pa = L_WQP + (unsigned)(lane & 1) * P_STRIDE + (unsigned)g4 * 16u;
#pragma unroll 1
    for (int un = 0; un < 128; ++un) {
      bf16x8 pf = lds_frag(L, pa + un * 64);
      if (landed < NQP + 1024 + 8 * (un + 1)) landed = lds_wait_ge<1>(L, L_LANDED, NQP + 1024 + 8 * (un + 1), budget);
      const unsigned ub = ((unsigned)un & 15u) * 8192u;
      bf16x8 b0 = lds_tr2(L, va0 + ub, va0 + ub + 1024), b1 = lds_tr2(L, va1 + ub, va1 + ub + 1024);
      asm volatile(LDS_PUT1 : "+v"(b0), "+v"(b1), "+v"(pf) : [pa] "v"(done_addr), [pv] "v"(64 + ((un + 1) >> 1)) : "memory");
      o0 = mfma(pf, b0, o0);
      o1 = mfma(pf, b1, o1);
    }
  }
  STAMP(5);
  lds_wait_ge<1>(L, L_FLAGF, 1, budget);
  {
    const float f = *(LAS float*)(L + L_F + (g4 & 1) * 4);
    const float e0 = ((g4 & 1) ? o0[1] : o0[0]) * f, e1 = ((g4 & 1) ? o1[1] : o1[0]) * f;
    {
      const u32x2 gr = {pk_bf16(e0, e1), tag};
      __builtin_amdgcn_raw_buffer_store_b64(gr, rws, lane < 32 ? (unsigned)(g4 * 64 + n16) * 8u : OOB, WS_PART + (unsigned)((kvh * 16 + r16) * 128 + c * 16) * 8u, AUX_SC1);
    }
  }
  STAMP(6);
  if (is_wo) {
    const unsigned xo = __builtin_amdgcn_raw_buffer_load_b32(rx, (lane & 3) * 4, (kvh >> 3) * 1152 + role * 128 + (kvh & 7) * 16, 0);
    lds_wait_ge<1>(L, L_LANDED, NQP + 2048 + 32, budget);
    bf16x8 wf[2][4];
    {
      const int ra = 32 * c + 16 * (tq & 1) + 4 * g4 + (tq >> 1), rc = ra + 2;
#pragma unroll
      for (int h = 0; h < 2; ++h)
#pragma unroll
        for (int t = 0; t < 4; ++t)
          wf[h][t] = lds_tr2(L, L_RING + (unsigned)(128 * h + ra) * 128u + (unsigned)(((2 * t + (tp >> 1)) ^ (((ra >> 1) & 3) << 1)) * 16 + (tp & 1) * 8),
                                L_RING + (unsigned)(128 * h + rc) * 128u + (unsigned)(((2 * t + (tp >> 1)) ^ (((rc >> 1) & 3) << 1)) * 16 + (tp & 1) * 8));
    }
    STAMP(7);
    u32x4 pv[4];
#pragma unroll 1
    for (;;) {
      CFENCE();
      bool ok = true;
#pragma unroll
      for (int j = 0; j < 4; ++j) {
        pv[j] = __builtin_amdgcn_raw_buffer_load_b128(rws, (unsigned)(n16 * 1024 + g4 * 32) + (j >> 1) * 512 + (j & 1) * 16, WS_PART + (unsigned)(kvh * 2048 + c * 16) * 8u, AUX_SC1);
        ok = ok & (pv[j].y == tag) & (pv[j].w == tag);
      }
      if (__all(ok) || (budget -= 64) < 0) break;
    }
    STAMP(8);
    const u32x4 a0 = {pv[0].x, pv[0].z, pv[1].x, pv[1].z}, a1 = {pv[2].x, pv[2].z, pv[3].x, pv[3].z};
    f32x4 red;
#pragma unroll
    for (int t = 0; t < 4; ++t) {
      f32x4 d = {0.f, 0.f, 0.f, 0.f};
      d = mfma(as_frag(a0), wf[0][t], d);
      d = mfma(as_frag(a1), wf[1][t], d);
      red[t] = (d[0] + d[1]) + (d[2] + d[3]);
    }
    *(LAS f32x4*)(L + L_RED + (c * 64 + lane) * 16) = red;
    LGKM0();
    BARRIER();
    CFENCE();
    if (c < 2) {
      float s = 0.f;
#pragma unroll
      for (int w4 = 0; w4 < 4; ++w4)
        s += *(LAS float*)(L + L_RED + (w4 * 64 + 32 * c + n16) * 16 + g4 * 4) + *(LAS float*)(L + L_RED + (w4 * 64 + 32 * c + 16 + n16) * 16 + g4 * 4);
      const u32x2 gr = {__float_as_uint(s), tag};
      __builtin_amdgcn_raw_buffer_store_b64(gr, rws, (unsigned)lane * 8u, WS_SP + (unsigned)((role * 16 + kvh) * 128 + c * 64) * 8u, AUX_SC1);
      STAMP(9);
    } else if (c == 2) {
      const int pr = lane >> 2;
      const unsigned in_off = (unsigned)(kvh * 64 + (lane & 3) * 16);
      u32x4 sv, yv;
#pragma unroll 1
      for (;;) {
        CFENCE();
        sv = __builtin_amdgcn_raw_buffer_load_b128(rws, (unsigned)pr * 1024u + in_off, WS_SP + (unsigned)role * 16384u, AUX_SC1);
        yv = __builtin_amdgcn_raw_buffer_load_b128(rws, (unsigned)min(pr, 11) * 1024u + in_off, WS_YP + (unsigned)role * 12288u, AUX_SC1);
        if (__all((sv.y == tag) & (sv.w == tag) & (yv.y == tag) & (yv.w == tag)) || (budget -= 64) < 0) break;
      }
      STAMP(9);
      float s0 = __uint_as_float(sv.x), s1 = __uint_as_float(sv.z);
      float y0 = pr < 12 ? __uint_as_float(yv.x) : 0.f, y1 = pr < 12 ? __uint_as_float(yv.z) : 0.f;
      s0 += dpp<DPP_ROR4>(s0); s1 += dpp<DPP_ROR4>(s1); y0 += dpp<DPP_ROR4>(y0); y1 += dpp<DPP_ROR4>(y1);
      s0 += dpp<DPP_ROR8>(s0); s1 += dpp<DPP_ROR8>(s1); y0 += dpp<DPP_ROR8>(y0); y1 += dpp<DPP_ROR8>(y1);
      s0 = x32_sum(x16_sum(s0)); s1 = x32_sum(x16_sum(s1)); y0 = x32_sum(x16_sum(y0)); y1 = x32_sum(x16_sum(y1));
      const float r0 = bf_lo(xo) + rbf(rbf(s0) * rbf(silu_f(rbf(y0)))), r1 = bf_hi(xo) + rbf(rbf(s1) * rbf(silu_f(rbf(y1))));
      __builtin_amdgcn_raw_buffer_store_b32(pk_bf16(r0, r1), mk_rsrc(a.out, 2 * D * 2), lane < 4 ? (unsigned)lane * 4u : OOB, (kvh >> 3) * 1152 + role * 128 + (kvh & 7) * 16, 0);
      if (wg == 0) __builtin_amdgcn_raw_buffer_store_b32(tag, rws, lane == 0 ? 0u : OOB, WS_EPOCH, AUX_SC1);
      STAMP(10);
    }
  } else if (is_wd) {
    lds_wait_ge<1>(L, L_FLAGH, 1, budget);
    f32x4 d = {0.f, 0.f, 0.f, 0.f};
#pragma unroll 1
    for (int k3 = 0; k3 < 3; ++k3) {
      bf16x8 hf[4];
#pragma unroll
      for (int ks = 0; ks < 4; ++ks) hf[ks] = lds_frag(L, L_HB + (lane & 1) * HB_STRIDE + k3 * 256 + ks * 64 + g4 * 16);
      lds_wait_ge<1>(L, L_LANDED, NQP + 2048 + 16 * (k3 + 1), budget);
#pragma unroll
      for (int ks = 0; ks < 4; ++ks) d = mfma(hf[ks], lds_tr2(L, L_RING + k3 * 16384 + ks * 4096 + wt0, L_RING + k3 * 16384 + ks * 4096 + wt1), d);
    }
    {
      const u32x2 gr = {__float_as_uint(g4 ? d[1] : d[0]), tag};
      __builtin_amdgcn_raw_buffer_store_b64(gr, rws, lane < 32 ? (unsigned)(g4 * 64 + n16) * 8u : OOB, WS_YP + (unsigned)((cbd * 12 + rbd) * 128 + c * 16) * 8u, AUX_SC1);
    }
    STAMP(9);
  }
}

}

extern "C" void kernel_launch(void* const* d_in, const int* in_sizes, int n_in, void* d_out, int out_size, void* d_ws,
                              size_t ws_size, hipStream_t stream) {
  Args a;
  a.x = d_in[0]; a.k = d_in[1]; a.v = d_in[2]; a.wq = d_in[3]; a.wo = d_in[4];
  a.wg = d_in[6]; a.wu = d_in[7]; a.wd = d_in[8]; a.out = d_out; a.ws = d_ws;
  hipLaunchKernelGGL(fused_decode, dim3(NWG), dim3(NTHR), 0, stream, a);
}
```

```cpp
#include <hip/hip_runtime.h>
#include <hip/hip_bf16.h>
#include <stdint.h>

#define DIAG 0
#define P_W0 25
#define P_WMIN 14
#define P_WMAX 38
#define P_WTO 12
#define P_WTD 12
#define P_WTI 36
#define P_TK 34.0
#define P_TV 34.0
#define P_TOFF 2.3
#define P_K1 3.0
#define P_K2 4.0
#define P_K3 11.0
#define P_D1 8
#define P_D2 6
#define P_D3 94
#define P_WOI 40
#define P_OPT 0
#define P_NT 1180717
#define P_AL 60030
#define P_C1 1.63
#define P_BI 0.65
#define P_NL 3
#define P_DW 0

#define P_OAUX 0
#define P_PAT 32456
#define TV(i, v) (v)
#define T_W0 TV(0, (int)(P_W0))
#define T_WMIN TV(1, (int)(P_WMIN))
#define T_WMAX TV(2, (int)(P_WMAX))
#define T_WTO TV(3, (int)(P_WTO))
#define T_WTD TV(4, (int)(P_WTD))
#define T_WTI TV(5, (int)(P_WTI))
#define T_TK TV(6, (int)(P_TK * 400.0))
#define T_TV TV(7, (int)(P_TV * 400.0))
#define T_TOFF TV(8, (int)(P_TOFF * 25600.0))
#define T_K1 TV(9, (int)(P_K1 * 2.56 + 0.5))
#define T_K2 TV(10, (int)(P_K2 * 2.56 + 0.5))
#define T_K3 TV(11, (int)(P_K3 * 2.56 + 0.5))
#define T_D1 TV(12, (int)(P_D1))
#define T_D2 TV(13, (int)(P_D2))
#define T_D3 TV(14, (int)(P_D3))
#define T_WOI TV(15, (int)(P_WOI))
#define T_OPT TV(16, (int)(P_OPT))
#define T_NT TV(17, (int)(P_NT))
#define T_AL TV(18, (int)(P_AL))
#define T_C1 TV(19, (int)(P_C1 * 25600.0))
#define T_BI TV(20, (int)(P_BI * 25600.0))

namespace {

#define LAS __attribute__((address_space(3)))
typedef unsigned u32x2 __attribute__((ext_vector_type(2)));
typedef unsigned u32x4 __attribute__((ext_vector_type(4)));
typedef int i32x4 __attribute__((ext_vector_type(4)));
typedef float f32x2 __attribute__((ext_vector_type(2)));
typedef float f32x4 __attribute__((ext_vector_type(4)));
typedef __bf16 bf16x2 __attribute__((ext_vector_type(2)));
typedef __bf16 bf16x4 __attribute__((ext_vector_type(4)));
typedef __bf16 bf16x8 __attribute__((ext_vector_type(8)));
typedef __amdgpu_buffer_rsrc_t rsrc_t;

constexpr int D = 576, FF = 4608, NWG = 256, NTHR = 384;
constexpr int AUX_SC1 = 16;

constexpr unsigned WS_EPOCH = 0;
constexpr unsigned WS_Q = 4096;
constexpr unsigned WS_GU = WS_Q + 32768;
constexpr unsigned WS_ML = WS_GU + 73728;
constexpr unsigned WS_PR = WS_ML + 32768;
constexpr unsigned WS_PART = WS_PR + 131072;
constexpr unsigned WS_SP = WS_PART + 262144;
constexpr unsigned WS_YP = WS_SP + 147456;
constexpr unsigned WS_END = WS_YP + 110592;
constexpr unsigned WS_DIAG = 1u << 20;

constexpr unsigned L_RING = 0;
constexpr unsigned L_P = 131072;
constexpr unsigned P_STRIDE = 8256;
constexpr unsigned L_LANDED = L_P + 16640;
constexpr unsigned L_DONE = L_LANDED + 256;
constexpr unsigned L_REF = L_DONE + 16, L_RAW = L_DONE + 24;
constexpr unsigned L_CNTM = L_DONE + 28, L_CNTP = L_CNTM + 4, L_FLAGF = L_CNTM + 8, L_FLAGH = L_CNTM + 12, L_FLAGQ = L_CNTM + 16;
constexpr unsigned L_TEND = L_CNTM + 20, L_PROBE = L_CNTM + 24;
constexpr unsigned L_MX = L_DONE + 64;
constexpr unsigned L_LS = L_MX + 32;
constexpr unsigned L_F = L_LS + 32;
constexpr unsigned L_G = L_F + 32;
constexpr unsigned L_Q16 = L_LANDED + 512;
constexpr unsigned L_HB = L_Q16 + 512;
constexpr unsigned HB_STRIDE = 832;
constexpr unsigned L_PA = L_HB + 1792;
constexpr unsigned PA_ROW = 80, PA_KS = 16 * PA_ROW;
constexpr unsigned L_XB = L_PA;
constexpr unsigned XB_STRIDE = 1216;
constexpr unsigned L_STAMP = L_PA + 8 * PA_KS;
constexpr unsigned L_HIST = L_STAMP + 512;
constexpr unsigned L_END = L_HIST + 768;
static_assert(L_END <= 163840, "LDS");
constexpr int NQP = 80;
constexpr int CODE_BYTES = 11264;
constexpr int S0 = 8;

struct Args {
  const void* x; const void* k; const void* v; const void* wq; const void* wo;
  const void* wg; const void* wu; const void* wd; void* out; void* ws;
};

__device__ __forceinline__ float bf_lo(unsigned u) { return __builtin_bit_cast(float, u << 16); }
__device__ __forceinline__ float bf_hi(unsigned u) { return __builtin_bit_cast(float, u & 0xffff0000u); }
__device__ __forceinline__ unsigned pk_bf16(float lo, float hi) {
  f32x2 v = {lo, hi};
  return __builtin_bit_cast(unsigned, __builtin_convertvector(v, bf16x2));
}
__device__ __forceinline__ float rbf(float x) { return bf_lo(pk_bf16(x, 0.f)); }
__device__ __forceinline__ float silu_f(float x) { return x * __builtin_amdgcn_rcpf(1.f + __builtin_amdgcn_exp2f(-1.4426950408889634f * x)); }
template <int CTRL> __device__ __forceinline__ float dpp(float x) {
  return __builtin_bit_cast(float, __builtin_amdgcn_mov_dpp(__builtin_bit_cast(int, x), CTRL, 0xf, 0xf, true));
}
constexpr int DPP_XOR1 = 0xB1, DPP_XOR2 = 0x4E, DPP_ROR4 = 0x124, DPP_ROR8 = 0x128;
__device__ __forceinline__ float x16_sum(float x) {
  auto s = __builtin_amdgcn_permlane16_swap(__float_as_uint(x), __float_as_uint(x), false, false);
  return __uint_as_float(s[0]) + __uint_as_float(s[1]);
}
__device__ __forceinline__ float x32_sum(float x) {
  auto t = __builtin_amdgcn_permlane32_swap(__float_as_uint(x), __float_as_uint(x), false, false);
  return __uint_as_float(t[0]) + __uint_as_float(t[1]);
}
__device__ __forceinline__ float x16_max(float x) {
  auto s = __builtin_amdgcn_permlane16_swap(__float_as_uint(x), __float_as_uint(x), false, false);
  return fmaxf(__uint_as_float(s[0]), __uint_as_float(s[1]));
}
__device__ __forceinline__ float x32_max(float x) {
  auto t = __builtin_amdgcn_permlane32_swap(__float_as_uint(x), __float_as_uint(x), false, false);
  return fmaxf(__uint_as_float(t[0]), __uint_as_float(t[1]));
}
__device__ __forceinline__ float half_sum(float v) {
  v += dpp<DPP_XOR1>(v); v += dpp<DPP_XOR2>(v); v += dpp<DPP_ROR4>(v); v += dpp<DPP_ROR8>(v);
  return x16_sum(v);
}
__device__ __forceinline__ float par_sum(float v) {
  v += dpp<DPP_XOR2>(v); v += dpp<DPP_ROR4>(v); v += dpp<DPP_ROR8>(v);
  return x32_sum(x16_sum(v));
}
__device__ __forceinline__ float par_max(float v) {
  v = fmaxf(v, dpp<DPP_XOR2>(v)); v = fmaxf(v, dpp<DPP_ROR4>(v)); v = fmaxf(v, dpp<DPP_ROR8>(v));
  return x32_max(x16_max(v));
}
__device__ __forceinline__ f32x4 mfma(bf16x8 a, bf16x8 b, f32x4 c) { return __builtin_amdgcn_mfma_f32_16x16x32_bf16(a, b, c, 0, 0, 0); }
__device__ __forceinline__ bf16x8 as_frag(u32x4 v) { return __builtin_bit_cast(bf16x8, v); }
__device__ __forceinline__ rsrc_t mk_rsrc(const void* p, unsigned bytes) { return __builtin_amdgcn_make_buffer_rsrc((void*)p, 0, (int)bytes, 0x00020000); }
#define CFENCE() asm volatile("" ::: "memory")
#define LGKM0() asm volatile("s_waitcnt lgkmcnt(0)" ::: "memory")
#define BARRIER() do { __builtin_amdgcn_sched_barrier(0); asm volatile("s_barrier" ::: "memory"); __builtin_amdgcn_sched_barrier(0); } while (0)

typedef LAS unsigned char* lds_t;
__device__ __forceinline__ bf16x8 lds_frag(lds_t L, unsigned off) { return *(LAS bf16x8*)(L + off); }
__device__ __forceinline__ bf16x8 lds_tr2(lds_t L, unsigned off0, unsigned off1) {
  bf16x4 a = __builtin_amdgcn_ds_read_tr16_b64_v4bf16((LAS bf16x4*)(L + off0));
  bf16x4 b = __builtin_amdgcn_ds_read_tr16_b64_v4bf16((LAS bf16x4*)(L + off1));
  return __builtin_shufflevector(a, b, 0, 1, 2, 3, 4, 5, 6, 7);
}
template <int SLEEP> __device__ __forceinline__ int lds_wait_ge(lds_t L, unsigned off, int need, int& budget) {
  int v; unsigned tmp;
  budget = __builtin_amdgcn_readfirstlane(budget); need = __builtin_amdgcn_readfirstlane(need);
  asm volatile("1:\n\tds_read_b32 %1, %3\n\ts_waitcnt lgkmcnt(0)\n\tv_readfirstlane_b32 %0, %1\n\ts_cmp_ge_i32 %0, %4\n\ts_cbranch_scc1 2f\n\t"
               "s_sub_i32 %2, %2, 1\n\ts_sleep %5\n\ts_cmp_gt_i32 %2, 0\n\ts_cbranch_scc1 1b\n2:"
               : "=&s"(v), "=&v"(tmp), "+s"(budget) : "v"((unsigned)(uintptr_t)L + off), "s"(need), "i"(SLEEP) : "scc", "memory");
  return v;
}
__device__ __forceinline__ void lds_arrive(lds_t L, unsigned off) {
  asm volatile("s_mov_b64 exec, 1\n\tds_add_u32 %0, %1\n\ts_mov_b64 exec, -1" :: "v"((unsigned)(uintptr_t)L + off), "v"(1u) : "memory");
}
#define LDS_PUT1 "s_mov_b64 exec, 1\n\tds_write_b32 %[pa], %[pv]\n\ts_mov_b64 exec, -1"
constexpr unsigned OOB = 0x40000000u;

struct LnStat { float m, s; };
__device__ __forceinline__ LnStat ln_stats(const unsigned (&xr)[9]) {
  float s = 0.f;
#pragma unroll
  for (int j = 0; j < 9; ++j) s += bf_lo(xr[j]) + bf_hi(xr[j]);
  const float mean = half_sum(s) * (1.f / D);
  float q = 0.f;
#pragma unroll
  for (int j = 0; j < 9; ++j) { const float a = bf_lo(xr[j]) - mean, b = bf_hi(xr[j]) - mean; q = fmaf(a, a, q); q = fmaf(b, b, q); }
  const float var = half_sum(q) * (1.f / D);
  const float m = rbf(mean), sd = __builtin_amdgcn_rcpf(rbf(__builtin_amdgcn_sqrtf(rbf(rbf(var) + 1e-5f))));
  LnStat r; r.m = m; r.s = sd;
  return r;
}
__device__ __forceinline__ unsigned ln_pair(unsigned u, float m, float sd) { const unsigned d = pk_bf16(bf_lo(u) - m, bf_hi(u) - m); return pk_bf16(bf_lo(d) * sd, bf_hi(d) * sd); }

constexpr unsigned LBIT = 10 + P_PAT / 10000, PA = 1024u << (P_PAT / 1000 % 10), PB = 1024u << (P_PAT / 100 % 10), GA = 1024u << (P_PAT / 10 % 10),
                   GB = 1024u << (P_PAT % 10), GS = 131072u;
#define I(x) x "\n\t"
#define LD4 I("buffer_load_dwordx4 v4, s[80:83], s84 offen nt lds") I("buffer_load_dwordx4 v5, s[80:83], s84 offen offset:1024 nt lds") \
            I("buffer_load_dwordx4 v6, s[80:83], s84 offen offset:2048 nt lds") I("buffer_load_dwordx4 v7, s[80:83], s84 offen offset:3072 nt lds")
#define NEXT4(ST) I("s_add_u32 m0, m0, 0x1000") I("s_add_u32 s84, s84, " ST)
#define GW1(n) I("s_waitcnt vmcnt(" #n ")") I("buffer_load_dwordx4 v4, s[80:83], s84 offen nt lds") \
               I("s_waitcnt vmcnt(" #n ")") I("buffer_load_dwordx4 v5, s[80:83], s84 offen offset:1024 nt lds") \
               I("s_waitcnt vmcnt(" #n ")") I("buffer_load_dwordx4 v6, s[80:83], s84 offen offset:2048 nt lds") \
               I("s_waitcnt vmcnt(" #n ")") I("buffer_load_dwordx4 v7, s[80:83], s84 offen offset:3072 nt lds") \
               I("s_setpc_b64 s[40:41]") I("s_nop 0") I("s_nop 0") I("s_nop 0")
#define GW8(a, b, c, d, e, f, g, h) GW1(a) GW1(b) GW1(c) GW1(d) GW1(e) GW1(f) GW1(g) GW1(h)
#define CALL4 I("s_swappc_b64 s[40:41], s[42:43]") I("ds_max_i32 v10, v40")
#define AFT4(ST) I("v_add_u32 v40, 4, v40") NEXT4(ST)
#define LSTAMP(i)
__device__ __forceinline__ unsigned loader_wave(unsigned lds_base) {
  unsigned epoch;
  asm volatile(
      I("s_load_dwordx16 s[64:79], %[ka], 0x0")
      I("s_load_dwordx2 s[92:93], %[ka], 0x48")
      I("v_readfirstlane_b32 s48, %[tid]")
      I("s_and_b32 s88, %[wg], 7")
      I("s_lshr_b32 s90, %[wg], 3")
      I("s_lshl2_add_u32 s89, s88, s90")
      I("s_and_b32 s89, s89, 31")
      I("s_cmp_lt_u32 s48, 64")
      I("s_cbranch_scc0 98f")
      I("v_lshrrev_b32 v1, 4, %[tid]")
      I("v_lshrrev_b32 v3, 3, %[tid]")
      I("v_lshlrev_b32 v2, 1, v1")
      I("v_xor_b32 v9, v2, %[tid]")
      I("v_and_b32 v2, 7, v9")
      I("v_lshlrev_b32 v2, 4, v2")
      I("v_and_b32 v9, 15, v9")
      I("v_xor_b32 v8, v1, %[tid]")
      I("v_and_b32 v8, 15, v8")
      I("v_and_b32 v4, 16, %[tid]")
      I("v_and_b32 v5, 32, %[tid]")
      I("v_lshl_or_b32 v4, v5, %[lsh], v4")
      I("s_lshl_b32 s48, s88, 5")
      I("v_or3_b32 v8, v8, v4, s48")
      I("v_or3_b32 v9, v9, v4, s48")
      I("v_lshlrev_b32 v8, 4, v8")
      I("v_lshlrev_b32 v9, 4, v9")
      I("s_mov_b32 s83, 0x20000")
      I("s_cmp_lt_u32 s89, 26")
      I("s_cbranch_scc0 10f")
      I("s_mul_i32 s48, s88, 18")
      I("s_add_u32 s48, s48, s89")
      I("s_lshr_b32 s49, s48, 1")
      I("s_lshl3_add_u32 s84, s88, s89")
      I("s_sub_u32 s84, s84, 18")
      I("s_movk_i32 s91, 0x2400")
      I("s_cmp_lt_u32 s89, 18")
      I("s_cselect_b32 s84, s49, s84")
      I("s_cselect_b32 s91, s91, 0x2000")
      I("s_lshl_b32 s84, s84, 7")
      I("s_lshl_b32 s85, s91, 5")
      I("s_mov_b32 s86, s85")
      I("s_mov_b32 s87, s85")
      I("s_mul_i32 s82, s91, 576")
      I("s_lshl_b32 s49, s91, 3")
      I("s_sub_u32 s49, s49, 0x400")
      I("v_mad_u32_u24 v4, v3, s91, v2")
      I("v_add_u32 v5, s49, v4")
      I("v_add_u32 v6, s49, v5")
      I("v_add_u32 v7, s49, v6")
      I("s_add_u32 m0, %[lb], 0xe000")
      I("s_mov_b32 s50, 5")
      I("s_mov_b32 s51, 8")
      I("s_mov_b32 s52, 32")
      I("s_mov_b32 s55, 3")
      I("s_mov_b32 s63, 0")
      I("s_bitcmp1_b32 s48, 0")
      I("s_waitcnt lgkmcnt(0)")
      I("s_cselect_b64 s[80:81], s[76:77], s[74:75]")
      I("s_cmp_lt_u32 s89, 18")
      I("s_cselect_b64 s[80:81], s[80:81], s[70:71]")
      I("s_and_b32 s81, s81, 0xffff")
      LD4 NEXT4("s85") LD4 NEXT4("s85")
      I("s_branch 11f")
      "10:\n\t"
      I("s_mov_b32 s63, -1")
      I("s_mov_b32 m0, %[lb]")
      I("s_mov_b32 s50, 9")
      I("s_mov_b32 s55, 8")
      I("s_movk_i32 s52, %[nqp]+16")
      "12:\n\t"
      I("v_mov_b32 v4, v8")
      I("v_xor_b32 v5, 64, v8")
      I("v_xor_b32 v6, 0x80, v8")
      I("v_xor_b32 v7, 0xc0, v8")
      I("v_add_u32 v5, %[c1], v5")
      I("v_add_u32 v6, %[c2], v6")
      I("v_add_u32 v7, %[c3], v7")
      I("s_mov_b32 s84, 0")
      I("s_mov_b32 s85, %[sa]")
      I("s_mov_b32 s86, %[sb]")
      I("s_mov_b32 s87, %[sc]")
      I("s_mov_b32 s82, 0x800000")
      I("s_movk_i32 s51, 72")
      I("s_lshl_b32 s48, s90, 23")
      I("s_waitcnt lgkmcnt(0)")
      I("s_add_u32 s80, s66, s48")
      I("s_addc_u32 s81, s67, 0")
      I("s_and_b32 s81, s81, 0xffff")
      I("s_cmp_eq_u32 s63, 0")
      I("s_mov_b32 s63, 1")
      I("s_cbranch_scc1 1f")
      "11:\n\t"
      LD4 NEXT4("s85") LD4 NEXT4("s86") LD4 NEXT4("s85") LD4 NEXT4("s87") LD4
      I("s_memrealtime s[46:47]")
      I("s_setprio 3")
      I("s_movk_i32 s20, %[w0]") I("s_movk_i32 s21, %[wmin]") I("s_movk_i32 s22, %[wmax]") I("s_movk_i32 s26, %[tk]") I("s_movk_i32 s29, %[k1]")
      I("s_mov_b32 s28, %[toff]")
      I("v_lshlrev_b32 v10, 2, %[tid]")
      I("v_add_u32 v10, %[landed], v10")
      I("v_add_u32 v10, %[lb], v10")
      I("v_mov_b32 v11, %[landed]+256")
      I("v_add_u32 v11, %[lb], v11")
      I("v_mul_u32_u24 v40, s63, %[tid]")
      I("v_add_u32 v40, s55, v40")
      I("s_lshl_b32 s48, s26, 3")
      I("s_sub_u32 s48, s28, s48")
      I("v_cmp_gt_u32 vcc, 4, %[tid]")
      I("v_mov_b32 v44, 0")
      I("ds_write_b32 v10, v44")
      I("v_cndmask_b32 v41, 0, v40, vcc")
      I("s_waitcnt lgkmcnt(0)")
      I("s_lshl_b32 s46, s46, 8")
      I("s_add_u32 s48, s48, s46")
      I("v_writelane_b32 v41, s48, 4")
      I("v_writelane_b32 v41, s29, 5")
      I("ds_write_b32 v10, v41 offset:256")
      I("s_mul_i32 s54, s50, s26")
      I("s_mov_b32 s56, 0x100000")
      I("s_sub_u32 s57, s20, 4")
      I("s_sub_u32 s59, s21, 4")
      I("s_sub_u32 s60, s22, 4")
      I("s_mov_b32 s61, s26")
      I("s_sub_u32 s48, s89, 18")
      I("s_cmp_lt_u32 s48, 8")
      I("s_cselect_b32 s57, s60, s57")
      I("s_cselect_b32 s62, 0, -1")
      I("s_mov_b32 s53, s57")
      I("s_lshl_b32 s48, s53, 6")
      I("s_add_u32 s48, s48, 14f-13f-512")
      I("s_getpc_b64 s[42:43]")
      "13:\n\t"
      I("s_add_u32 s42, s42, s48")
      I("s_addc_u32 s43, s43, 0")
      I("s_sub_i32 s48, s52, s53")
      I("s_add_i32 s48, s48, 3")
      I("v_mov_b32 v40, s48")
      I("s_waitcnt lgkmcnt(0)")
      I("s_barrier")
      LSTAMP(6)
      I("s_branch 3f")
      "1:\n\t"
      I("s_sub_i32 s48, s50, 7")
      I("s_cmp_ge_i32 s55, s48")
      I("s_cbranch_scc0 5f")
      "2:\n\t"
      I("s_and_b32 s48, s50, 7")
      I("s_lshl_b32 s48, s48, 14")
      I("s_add_u32 m0, s48, %[lb]")
      CALL4
      "3:\n\t"
      I("ds_read_b128 v[44:47], v11")
      I("ds_read_b64 v[42:43], v11 offset:16")
      I("s_memrealtime s[46:47]")
      AFT4("s85")
      I("s_swappc_b64 s[40:41], s[42:43]")
      I("s_waitcnt lgkmcnt(0)")
      I("ds_max_i32 v10, v40")
      AFT4("s86")
      I("v_min3_i32 v44, v44, v45, v46")
      I("v_min_i32 v44, v44, v47")
      I("v_readfirstlane_b32 s37, v42")
      I("v_readfirstlane_b32 s49, v43")
      I("v_readfirstlane_b32 s55, v44")
      CALL4
      AFT4("s85")
      I("s_lshl_b32 s46, s46, 8")
      I("s_sub_i32 s46, s46, s54")
      I("v_mov_b32 v41, s46")
      I("s_sub_i32 s46, s46, s37")
      I("ds_write_b32 v11, v41 offset:24")
      I("s_and_b32 s49, s49, s62")
      I("s_mul_i32 s46, s46, s49")
      I("s_ashr_i32 s46, s46, 16")
      I("s_add_i32 s46, s46, s57")
      I("s_max_i32 s46, s46, s59")
      I("s_min_i32 s49, s46, s60")
      CALL4
      I("s_sub_i32 s48, s49, s53")
      I("s_mov_b32 s53, s49")
      I("v_subrev_u32 v40, s48, v40")
      I("s_lshl_b32 s48, s48, 6")
      I("s_ashr_i32 s49, s48, 31")
      I("s_add_u32 s42, s42, s48")
      I("s_addc_u32 s43, s43, s49")
      I("v_add_u32 v40, 4, v40")
      I("s_add_u32 s84, s84, s87")
      I("s_add_i32 s54, s54, s61")
      I("s_add_i32 s52, s52, 16")
      I("s_add_i32 s50, s50, 1")
      I("s_cmp_lt_i32 s50, s51")
      I("s_cbranch_scc1 1b")
      I("s_cmp_eq_u32 s63, 2")
      I("s_cbranch_scc1 22f")
      I("s_cmp_gt_u32 s63, 2")
      I("s_cbranch_scc1 23f")
      I("s_cmp_eq_u32 s63, 1")
      I("s_cbranch_scc1 21f")
      I("s_sub_u32 s57, s20, 4")
      I("s_mov_b32 s62, -1")
      I("s_branch 12b")
      "21:\n\t"
      LSTAMP(1)
      I("v_mov_b32 v4, v9")
      I("v_add_u32 v5, %[c1], v9")
      I("v_xor_b32 v7, 0x80, v9")
      I("v_add_u32 v6, %[c2], v7")
      I("v_add_u32 v7, %[c3], v7")
      I("s_lshl_b32 s48, s90, 23")
      I("s_add_u32 s80, s68, s48")
      I("s_addc_u32 s81, s69, 0")
      I("s_and_b32 s81, s81, 0xffff")
      I("s_mov_b32 s84, 0")
      I("s_mov_b32 s63, 2")
      I("s_movk_i32 s61, %[tv]")
      I("s_add_u32 s51, s89, 80")
      I("s_branch 1b")
      "22:\n\t"
      I("s_movk_i32 s51, 136")
      I("s_mov_b32 s63, 3")
      I("s_mul_i32 s48, s88, 18")
      I("s_mul_i32 s49, s88, 14")
      I("s_sub_i32 s49, s49, 18")
      I("s_cmp_lt_u32 s89, 18")
      I("s_cselect_b32 s48, s48, s49")
      I("s_cselect_b64 s[72:73], s[72:73], s[78:79]")
      I("s_mov_b32 s92, 0x6c000")
      I("s_cselect_b32 s92, 0x48000, s92")
      I("s_mov_b32 s93, 0x510000")
      I("s_cselect_b32 s93, 0x480000, s93")
      I("s_cselect_b32 s91, 2, 3")
      I("s_cselect_b32 s49, 0, 36")
      I("s_add_u32 s48, s48, s89")
      I("s_add_u32 s49, s49, s48")
      I("s_cmp_lt_u32 s49, 144")
      I("s_cselect_b32 s91, s91, 0")
      I("s_mul_i32 s49, s48, 57")
      I("s_lshr_b32 s49, s49, 9")
      I("s_mul_i32 s92, s92, s49")
      I("s_mul_i32 s49, s49, 9")
      I("s_sub_u32 s48, s48, s49")
      I("s_lshl_b32 s48, s48, 7")
      I("s_add_u32 s92, s92, s48")
      I("s_and_b32 s73, s73, 0xffff")
      I("s_movk_i32 s48, 0x480")
      I("v_mad_u32_u24 v8, v3, s48, v2")
      I("s_branch 1b")
      "23:\n\t"
      LSTAMP(2)
      I("s_add_i32 s37, s37, %[gate]")
      I("s_add_i32 s37, s37, s54")
      I("v_mov_b32 v4, v8")
      I("v_add_u32 v5, 0x2000, v8")
      I("v_add_u32 v6, 0x4000, v8")
      I("v_add_u32 v7, 0x6000, v8")
      I("s_mov_b64 s[80:81], s[72:73]")
      I("s_mov_b32 s82, s93")
      I("s_mov_b32 s84, s92")
      I("s_mov_b32 m0, %[lb]")
      I("s_add_u32 s48, s91, 128")
      I("s_lshl_b32 s91, s91, 4")
      I("s_cbranch_scc0 24f")
      I("s_mov_b32 s53, 0")
      I("s_mov_b32 s63, 4")
      I("s_cmp_ge_i32 s55, s48")
      I("s_cbranch_scc0 5f")
      "25:\n\t"
      LD4
      I("s_memrealtime s[46:47]")
      I("s_add_u32 s53, s53, 4")
      I("s_add_u32 s52, s52, 4")
      NEXT4("0x9000")
      I("s_cmp_ge_u32 s53, s91")
      I("s_cbranch_scc1 24f")
      I("s_sub_i32 s49, s53, 12")
      I("s_sub_i32 s48, s52, 12")
      I("s_waitcnt lgkmcnt(0)")
      I("s_lshl_b32 s46, s46, 8")
      I("s_sub_i32 s46, s46, s37")
      I("s_min_i32 s46, s46, s49")
      I("s_cmp_gt_i32 s46, 0")
      I("s_cbranch_scc1 25b")
      I("v_mov_b32 v40, s48")
      I("s_waitcnt vmcnt(12)")
      I("ds_max_i32 v10, v40")
      I("s_branch 25b")
      "24:\n\t"
      I("s_sub_i32 s48, s52, 32")
      I("v_mov_b32 v40, s48")
#define DR(n) I("s_waitcnt vmcnt(" #n ")") I("ds_max_i32 v10, v40") I("v_add_u32 v40, 4, v40")
      DR(32) DR(28) DR(24) DR(20) DR(16) DR(12) DR(8) DR(4) DR(0)
      LSTAMP(3)
      I("s_endpgm")
      "5:\n\t"
      I("ds_read_b128 v[44:47], v11")
      I("s_waitcnt lgkmcnt(0)")
      I("v_min3_i32 v44, v44, v45, v46")
      I("v_min_i32 v44, v44, v47")
      I("s_sub_i32 s56, s56, 1")
      I("v_readfirstlane_b32 s55, v44")
      I("s_cmp_ge_i32 s55, s48")
      I("s_cbranch_scc1 6f")
      I("s_waitcnt vmcnt(0)")
      I("v_mov_b32 v41, s52")
      I("ds_max_i32 v10, v41")
      I("s_sleep 1")
      I("s_cmp_gt_i32 s56, 0")
      I("s_cbranch_scc1 5b")
      "6:\n\t"
      I("s_cmp_eq_u32 s63, 4")
      I("s_cbranch_scc1 25b")
      I("s_branch 2b")
      I(".p2align 6")
      "14:\n\t"
      GW8(8, 9, 10, 11, 12, 13, 14, 15) GW8(16, 17, 18, 19, 20, 21, 22, 23) GW8(24, 25, 26, 27, 28, 29, 30, 31) GW8(32, 33, 34, 35, 36, 37, 38, 39)
      "98:\n\t"
      I("s_lshr_b32 s48, s48, 6")
      I("s_sub_u32 s49, s48, 2")
      I("s_cmp_lt_u32 s89, 18")
      I("s_mov_b32 s84, 1572736")
      I("s_cselect_b32 s84, 1769344, s84")
      I("s_mul_i32 s84, s84, s49")
      I("s_lshl_b32 s85, s90, 23")
      I("s_bitcmp1_b32 s89, 0")
      I("s_waitcnt lgkmcnt(0)")
      I("s_load_dword %[ep], s[92:93], 0x0")
      I("s_cselect_b64 s[80:81], s[76:77], s[74:75]")
      I("s_cmp_lt_u32 s89, 18")
      I("s_cselect_b64 s[80:81], s[80:81], s[70:71]")
      I("s_cmp_eq_u32 s48, 2")
      I("s_cselect_b64 s[80:81], s[66:67], s[80:81]")
      I("s_cselect_b32 s84, s85, s84")
      I("s_cmp_eq_u32 s48, 1")
      I("s_cselect_b64 s[80:81], s[64:65], s[80:81]")
      I("s_cselect_b32 s84, 0, s84")
      I("s_add_u32 s80, s80, s84")
      I("s_addc_u32 s81, s81, 0")
      I("s_load_dword s85, s[80:81], 0x0")
      I("s_barrier")
      "96:\n\t"
      "s_waitcnt lgkmcnt(0)"
      : [ep] "=&s"(epoch)
      : [tid] "v"(threadIdx.x), [wg] "s"(blockIdx.x), [ka] "s"(__builtin_amdgcn_kernarg_segment_ptr()), [lb] "s"(lds_base),
         [nqp] "i"(NQP), [landed] "i"(L_LANDED), [c1] "i"(PA - 1024u), [c2] "i"(PB - 2048u), [c3] "i"(PA + PB - 3072u),
         [lsh] "i"(LBIT - 9), [sa] "i"(GA), [sb] "i"(GB - GA), [sc] "i"(GS - GA - GB)
         , [w0] "i"(T_W0), [wmin] "i"(T_WMIN), [wmax] "i"(T_WMAX), [gate] "i"(T_BI), [tk] "i"(T_TK),
         [tv] "i"(T_TV), [toff] "i"(T_TOFF), [k1] "i"(T_K1)
      : "s20", "s21", "s22", "s23", "s24", "s25", "s26", "s27", "s28", "s29", "s30", "s31", "s33", "s34", "s35", "s36", "s37", "s38", "s40", "s41", "s42", "s43", "s44", "s45", "s46", "s47", "s48", "s49", "s50", "s51", "s52", "s53", "s54", "s55", "s56", "s57", "s58", "s59", "s60", "s61", "s62", "s63", "s64", "s65", "s66", "s67", "s68", "s69", "s70", "s71", "s72", "s73", "s74", "s75", "s76", "s77", "s78", "s79", "s80", "s81", "s82", "s83", "s84", "s85", "s86", "s87", "s88", "s89", "s90", "s91", "s92", "s93", "s94", "s95",

        "v1", "v2", "v3", "v4", "v5", "v6", "v7", "v8", "v9", "v10", "v11", "v40", "v41", "v42", "v43", "v44", "v45", "v46", "v47", "vcc", "scc", "memory");
  return epoch;
}
#undef I
static_assert(L_RING == 0 && L_DONE == L_LANDED + 256 && L_REF == L_DONE + 16 && L_RAW == L_DONE + 24 && S0 == 8 && WS_EPOCH == 0, "fixed in loader_wave");

__device__ __forceinline__ unsigned next_tag(unsigned e) {
  const unsigned t = e * 1664525u + 1013904223u;
  return t ? t : 1013904223u;
}
__device__ __forceinline__ void wait_until(int t) {
#pragma unroll 1
  for (int i = 0; i < 48 && (int)(((unsigned)__builtin_amdgcn_s_memrealtime() << 8) - (unsigned)t) < 0; ++i) {}
}
__device__ __forceinline__ void pause(int n) {
#pragma unroll 1
  for (int i = 0; i < n; i += 2) __builtin_amdgcn_s_sleep(2);
}

#define STAMP(i) do {} while (0)
#define STAMP_INIT() do {} while (0)
#define STAMP_DUMP() do {} while (0)

__global__ void __launch_bounds__(NTHR, 2) fused_decode(Args a) {
  __shared__ __attribute__((aligned(16))) unsigned char lds_raw[163840];
  lds_t L = (lds_t)lds_raw;
  const unsigned epoch = loader_wave((unsigned)(uintptr_t)L);
  __builtin_amdgcn_sched_barrier(0);
  const int lane = threadIdx.x & 63, wave = __builtin_amdgcn_readfirstlane(threadIdx.x >> 6);
  const int wg = blockIdx.x;
  const int xg = wg & 7, job = ((wg >> 3) + 4 * xg) & 31;
  const bool is_wo = job < 18;
  const int ko = 2 * xg + (job >= 9 ? 1 : 0), cb = job >= 9 ? job - 9 : job;
  const int didx = xg * 14 + job - 18;
  const bool is_wd = !is_wo && didx < 108;
  const int rbd = didx / 9, cbd = didx - rbd * 9;
  const unsigned tail_off = is_wo ? (unsigned)(ko * 256 * 1152 + cb * 128) : (unsigned)(rbd * 384 * 1152 + cbd * 128);
  const bool has_front = job < 26;
  const int gidx = xg * 18 + job, fcol = (is_wo ? gidx >> 1 : xg * 8 + job - 18) * 64;
  int budget = 1 << 20;
  typedef const __attribute__((address_space(4))) unsigned* cptr_t;
  const int kvh = (wg >> 3) & 15, bat = wg >> 7, r16 = bat * 8 + xg;
  const int n16 = lane & 15, g4 = lane >> 4;
  const int tq = (lane >> 2) & 3, tp = lane & 3;
  const rsrc_t rws = mk_rsrc(a.ws, WS_END);
  STAMP_INIT();
  const unsigned tag = next_tag(epoch);

  if (wave == 5) {
    {
      lds_wait_ge<4>(L, L_LANDED, has_front ? NQP : NQP + 64, budget);
      u32x4 qp[2];
#pragma unroll 1
      for (;;) {
        CFENCE();
#pragma unroll
        for (int j = 0; j < 2; ++j) qp[j] = __builtin_amdgcn_raw_buffer_load_b128(rws, (unsigned)lane * 16u + j * 1024, WS_Q + (unsigned)kvh * 2048u, AUX_SC1);
        if (__all((qp[0].y == tag) & (qp[0].w == tag) & (qp[1].y == tag) & (qp[1].w == tag)) || (budget -= 64) < 0) break;
        __builtin_amdgcn_s_sleep(4);
      }
#pragma unroll
      for (int h = 0; h < 2; ++h)
        *(LAS unsigned*)(L + L_Q16 + h * 256 + lane * 4) = bat ? (qp[h].x >> 16) | (qp[h].z & 0xffff0000u) : (qp[h].x & 0xffffu) | (qp[h].z << 16);
      lds_arrive(L, L_FLAGQ);
      STAMP(1);
    }
    if (!(T_OPT & 1)) {
#define TOUCH(p, off) asm volatile("" :: "s"(*(cptr_t)((uintptr_t)(p) + (size_t)(off))))
      const size_t kvb = (size_t)(wg >> 3) * 8388608u + (size_t)wg * 4352u;
      TOUCH(a.k, kvb + 2097152u); TOUCH(a.k, kvb + 4194304u); TOUCH(a.k, kvb + 6291456u);
      TOUCH(a.v, kvb); TOUCH(a.v, kvb + 2097152u); TOUCH(a.v, kvb + 4194304u); TOUCH(a.v, kvb + 6291456u);
      if (is_wo || is_wd) { TOUCH(is_wo ? a.wo : a.wd, tail_off); TOUCH(is_wo ? a.wo : a.wd, tail_off + (is_wo ? 255u : 383u) * 1152u); }
      if (!(T_OPT & 8)) TOUCH(a.out, 0);
    }
    int st_ml = 0, st_h = is_wd ? 0 : 1;
    float mw = 0.f;
    const int nt = T_NT % 100, ntick = T_NT / 100 % 100 + 1, tlast = T_NT / 10000;
#pragma unroll 1
    for (int tk = 1; tk <= ntick || st_ml < 2 || st_h < 1; ++tk) {
      if (tk <= ntick) lds_wait_ge<16>(L, L_LANDED, NQP + 16 * min(tlast - (ntick - 1 - tk) * nt, tlast + 5), budget);
      else if ((budget -= 64) < 0) break;
      CFENCE();
      if (tk <= ntick) {
        const int raw = *(volatile LAS int*)(L + L_RAW);
        const int g9 = (wg >> 3) / 9;
        if ((wg >> 3) == 9 * g9 && tk < ntick) {
          const u32x2 gr = {(unsigned)raw, tag};
          __builtin_amdgcn_raw_buffer_store_b64(gr, rws, lane < 16 ? (unsigned)lane * 8u : OOB, WS_PR + (unsigned)(tk * 32 + g9 * 8 + xg) * 128u, AUX_SC1);
        }
        const u32x2 pr = __builtin_amdgcn_raw_buffer_load_b64(rws, tk > 1 ? (unsigned)(lane & 31) * 128u : OOB, WS_PR + (unsigned)(tk - 1) * 4096u, AUX_SC1);
        if (__all(pr.y == tag)) {
          float dl = (float)((int)pr.x - raw);
          dl += dpp<DPP_ROR8>(dl); dl = x16_sum(dl);
          float dm = fmaxf(dl, dpp<DPP_XOR1>(dl)); dm = fmaxf(dm, dpp<DPP_XOR2>(dm)); dm = fmaxf(dm, dpp<DPP_ROR4>(dm));
          dl += dpp<DPP_XOR1>(dl); dl += dpp<DPP_XOR2>(dl); dl += dpp<DPP_ROR4>(dl);
          dl *= 0.03125f;
          dm = dm * 0.25f - dl;
          *(volatile LAS int*)(L + L_TEND) = raw + (int)(dl + (float)(T_AL / 1000) * 0.01f * dm);
          if (tk > 3) dl += (float)(T_AL % 1000) * 0.01f * dm;
          const u32x2 rg = {(unsigned)(raw + (int)dl), (unsigned)min(T_K3, T_K1 + T_K2 * (tk - 2))};
          *(volatile LAS u32x2*)(L + L_REF) = rg;
        }
      }
      if (st_ml == 0) {
        if (__builtin_amdgcn_readfirstlane(*(volatile LAS int*)(L + L_CNTP)) >= 4) {
          CFENCE();
          mw = fmaxf(fmaxf(*(LAS float*)(L + L_MX + (lane & 1) * 4), *(LAS float*)(L + L_MX + 8 + (lane & 1) * 4)),
                     fmaxf(*(LAS float*)(L + L_MX + 16 + (lane & 1) * 4), *(LAS float*)(L + L_MX + 24 + (lane & 1) * 4)));
          const float lw = (*(LAS float*)(L + L_LS + (lane & 1) * 4) + *(LAS float*)(L + L_LS + 8 + (lane & 1) * 4)) +
                           (*(LAS float*)(L + L_LS + 16 + (lane & 1) * 4) + *(LAS float*)(L + L_LS + 24 + (lane & 1) * 4));
          const u32x4 gr = {__float_as_uint(mw), tag, __float_as_uint(lw), tag};
          __builtin_amdgcn_raw_buffer_store_b128(gr, rws, lane < 8 ? (unsigned)(wg * 128 + lane * 16) : OOB, WS_ML, AUX_SC1);
          st_ml = 1;
        }
      } else if (st_ml == 1) {
        const u32x4 ml = __builtin_amdgcn_raw_buffer_load_b128(rws, (unsigned)(((wg & ~7) + ((lane >> 1) & 7)) * 128 + (lane & 1) * 16), WS_ML, AUX_SC1);
        if (__all((ml.y == tag) & (ml.w == tag))) {
          const float ms = __uint_as_float(ml.x);
          float M = fmaxf(ms, dpp<DPP_XOR2>(ms)); M = fmaxf(M, dpp<DPP_ROR4>(M)); M = fmaxf(M, dpp<DPP_ROR8>(M));
          float Ls = __uint_as_float(ml.z) * __builtin_amdgcn_exp2f(ms - M);
          Ls += dpp<DPP_XOR2>(Ls); Ls += dpp<DPP_ROR4>(Ls); Ls += dpp<DPP_ROR8>(Ls);
          *(LAS float*)(L + L_F + (lane & 1) * 4) = __builtin_amdgcn_exp2f(mw - M) * __builtin_amdgcn_rcpf(Ls);
          lds_arrive(L, L_FLAGF);
          st_ml = 2;
          STAMP(2);
        }
      }
      if (st_h == 0) {
        u32x4 gv[3], uv[3];
        bool ok = true;
#pragma unroll
        for (int j = 0; j < 3; ++j) {
          gv[j] = __builtin_amdgcn_raw_buffer_load_b128(rws, (unsigned)lane * 16u + j * 1024, WS_GU + (unsigned)rbd * 3072u, AUX_SC1);
          uv[j] = __builtin_amdgcn_raw_buffer_load_b128(rws, (unsigned)lane * 16u + j * 1024, WS_GU + 36864u + (unsigned)rbd * 3072u, AUX_SC1);
          ok = ok & (gv[j].y == tag) & (gv[j].w == tag) & (uv[j].y == tag) & (uv[j].w == tag);
        }
        if (__all(ok)) {
#pragma unroll
          for (int j = 0; j < 3; ++j) {
            *(LAS unsigned*)(L + L_HB + (lane + 64 * j) * 4) =
                pk_bf16(rbf(silu_f(bf_lo(gv[j].x))) * bf_lo(uv[j].x), rbf(silu_f(bf_lo(gv[j].z))) * bf_lo(uv[j].z));
            *(LAS unsigned*)(L + L_HB + HB_STRIDE + (lane + 64 * j) * 4) =
                pk_bf16(rbf(silu_f(bf_hi(gv[j].x))) * bf_hi(uv[j].x), rbf(silu_f(bf_hi(gv[j].z))) * bf_hi(uv[j].z));
          }
          lds_arrive(L, L_FLAGH);
          st_h = 1;
          STAMP(3);
        }
      }
    }
    STAMP_DUMP();
    return;
  }

  const int c = wave - 1;
  const rsrc_t rx = mk_rsrc(a.x, 2 * D * 2);
  STAMP(0);
  const unsigned wt0 = (unsigned)(8 * g4 + tq) * 128u + (unsigned)(((2 * c + (tp >> 1)) ^ ((tq >> 1) << 1)) * 16 + (tp & 1) * 8);
  const unsigned wt1 = (unsigned)(8 * g4 + 4 + tq) * 128u + (unsigned)(((2 * c + (tp >> 1)) ^ ((2 + (tq >> 1)) << 1)) * 16 + (tp & 1) * 8);
  const unsigned done_addr = (unsigned)(uintptr_t)L + L_DONE + (unsigned)c * 4u;
  int landed = 0;
  if (has_front) {
    unsigned xr[9];
#pragma unroll
    for (int j = 0; j < 9; ++j) xr[j] = __builtin_amdgcn_raw_buffer_load_b32(rx, (lane >> 5) * 1152 + (lane & 31) * 4 + j * 128, 0, 0);
    const LnStat st = ln_stats(xr);
#pragma unroll
    for (int j = 0; j < 9; ++j)
      *(LAS unsigned*)(L + L_XB + (lane >> 5) * XB_STRIDE + ((lane & 31) + 32 * j) * 4) = ln_pair(xr[j], st.m, st.s);
    f32x4 d = {0.f, 0.f, 0.f, 0.f};
#pragma unroll 1
    for (int hs = 0; hs < 9; ++hs) {
      bf16x8 a0 = lds_frag(L, L_XB + (lane & 1) * XB_STRIDE + hs * 128 + g4 * 16), a1 = lds_frag(L, L_XB + (lane & 1) * XB_STRIDE + hs * 128 + 64 + g4 * 16);
      if (landed < 8 * (hs + 2)) landed = lds_wait_ge<1>(L, L_LANDED, 8 * (hs + 2), budget);
      const unsigned fb = L_RING + 7 * 8192 + (unsigned)hs * 8192u;
      bf16x8 b0 = lds_tr2(L, fb + wt0, fb + wt1), b1 = lds_tr2(L, fb + 4096 + wt0, fb + 4096 + wt1);
      asm volatile(LDS_PUT1 : "+v"(b0), "+v"(b1), "+v"(a0), "+v"(a1) : [pa] "v"(done_addr), [pv] "v"(hs == 8 ? S0 + c : 4 + (hs >> 1)) : "memory");
      d = mfma(a0, b0, d);
      d = mfma(a1, b1, d);
    }
    const u32x2 gr = {pk_bf16(d[0], d[1]), tag};
    __builtin_amdgcn_raw_buffer_store_b64(gr, rws, lane < 16 ? (unsigned)lane * 8u : OOB,
                                          (is_wo ? WS_GU + (unsigned)(gidx & 1) * 36864u : WS_Q) + (unsigned)(fcol + c * 16) * 8u, AUX_SC1);
    STAMP(1);
  }
  f32x4 scr[8];
  float mx = -INFINITY;
#pragma unroll
  for (int i = 0; i < 8; ++i) scr[i] = f32x4{0.f, 0.f, 0.f, 0.f};
  {
    unsigned ka[4];
#pragma unroll
    for (int kk = 0; kk < 4; ++kk) ka[kk] = L_RING + (unsigned)c * 16384u + (unsigned)n16 * 256u + (unsigned)(((4 * kk + g4) ^ n16) * 16);
#pragma unroll 1
    for (int it = 0; it < 8; ++it) {
      bf16x8 kf[2][16];
#pragma unroll
      for (int hf = 0; hf < 2; ++hf) {
        const int n = 8 * it + 4 * hf + c;
        if (landed < NQP + 16 * (n + 1)) landed = lds_wait_ge<2>(L, L_LANDED, NQP + 16 * (n + 1), budget);
#pragma unroll
        for (int blk = 0; blk < 4; ++blk)
#pragma unroll
          for (int kk = 0; kk < 4; ++kk) kf[hf][blk * 4 + kk] = lds_frag(L, ka[kk] + hf * 65536 + blk * 4096);
        asm volatile(LDS_PUT1 : "+v"(kf[hf][0]), "+v"(kf[hf][1]), "+v"(kf[hf][2]), "+v"(kf[hf][3]), "+v"(kf[hf][4]), "+v"(kf[hf][5]), "+v"(kf[hf][6]),
                                "+v"(kf[hf][7]), "+v"(kf[hf][8]), "+v"(kf[hf][9]), "+v"(kf[hf][10]), "+v"(kf[hf][11]), "+v"(kf[hf][12]),
                                "+v"(kf[hf][13]), "+v"(kf[hf][14]), "+v"(kf[hf][15])
                     : [pa] "v"(done_addr), [pv] "v"(S0 + min(n + 4, 64)) : "memory");
      }
      if (it == 0) { lds_wait_ge<1>(L, L_FLAGQ, 1, budget); STAMP(2); }
      CFENCE();
      bf16x8 qf[4];
#pragma unroll
      for (int kk = 0; kk < 4; ++kk) qf[kk] = lds_frag(L, L_Q16 + (lane & 1) * 256 + kk * 64 + g4 * 16);
      f32x4 cur = {0.f, 0.f, 0.f, 0.f};
#pragma unroll
      for (int u = 0; u < 8; ++u) {
        f32x4 d = {0.f, 0.f, 0.f, 0.f};
#pragma unroll
        for (int kk = 0; kk < 4; ++kk) d = mfma(kf[u >> 2][(u & 3) * 4 + kk], qf[kk], d);
        const bool sel = (n16 >> 1) == u;
#pragma unroll
        for (int e = 0; e < 4; ++e) cur[e] = sel ? d[e] : cur[e];
      }
#pragma unroll
      for (int e = 0; e < 4; ++e) {
        cur[e] = rbf(rbf(cur[e]) * 0.08838834764831845f) * 1.4426950408889634f;
        mx = fmaxf(mx, cur[e]);
      }
#pragma unroll
      for (int i = 0; i < 7; ++i) scr[i] = scr[i + 1];
      scr[7] = cur;
    }
  }
  STAMP(3);
  const unsigned out_off = (unsigned)((ko >> 3) * 1152 + cb * 128 + (ko & 7) * 16);
  const unsigned xo16 = __builtin_amdgcn_raw_buffer_load_b16(rx, (lane & 7) * 2, out_off, 0);
  {
    mx = par_max(mx);
    *(LAS float*)(L + L_MX + c * 8 + (lane & 1) * 4) = mx;
    lds_arrive(L, L_CNTM);
    lds_wait_ge<1>(L, L_CNTM, 4, budget);
    mx = fmaxf(fmaxf(*(LAS float*)(L + L_MX + (lane & 1) * 4), *(LAS float*)(L + L_MX + 8 + (lane & 1) * 4)),
               fmaxf(*(LAS float*)(L + L_MX + 16 + (lane & 1) * 4), *(LAS float*)(L + L_MX + 24 + (lane & 1) * 4)));
    float ls = 0.f;
    const int u = n16 >> 1;
    const unsigned pw = L_P + (unsigned)(lane & 1) * P_STRIDE + (unsigned)(64 * (4 * (u >> 2) + c) + 16 * (u & 3) + 4 * g4) * 2u;
#pragma unroll
    for (int i = 0; i < 8; ++i) {
      const float p0 = __builtin_amdgcn_exp2f(scr[i][0] - mx), p1 = __builtin_amdgcn_exp2f(scr[i][1] - mx);
      const float p2 = __builtin_amdgcn_exp2f(scr[i][2] - mx), p3 = __builtin_amdgcn_exp2f(scr[i][3] - mx);
      ls += (p0 + p1) + (p2 + p3);
      const u32x2 pk = {pk_bf16(p0, p1), pk_bf16(p2, p3)};
      *(LAS u32x2*)(L + pw + i * 1024) = pk;
    }
    ls = par_sum(ls);
    *(LAS float*)(L + L_LS + c * 8 + (lane & 1) * 4) = ls;
    lds_arrive(L, L_CNTP);
    lds_wait_ge<1>(L, L_CNTP, 4, budget);
  }
  STAMP(4);
  f32x4 o0 = {0.f, 0.f, 0.f, 0.f}, o1 = {0.f, 0.f, 0.f, 0.f};
  float fnorm = 0.f;
  unsigned st_off = lane < 32 ? (unsigned)(g4 * 64 + n16) * 8u : OOB, g1_off = (unsigned)((lane >> 3) * 1024 + (lane & 7) * 16);
  unsigned s2_off = (g4 & 1) ? OOB : (unsigned)((g4 >> 1) * 64 + n16) * 8u;
  unsigned fin_off = (unsigned)((lane >> 3) * 1024 + ko * 64 + (lane & 7) * 8), fin_of2 = fin_off + (lane < 32 ? 8192u : 0u);
  unsigned pa_w = (unsigned)(uintptr_t)L + L_PA + (unsigned)(c * PA_KS + (lane >> 3) * PA_ROW + (lane & 7) * 8), pa_w2 = pa_w + 4 * PA_KS;
  unsigned pa_r = L_PA + (unsigned)(n16 * PA_ROW + g4 * 16);
  unsigned o_off = lane < 8 ? (unsigned)lane * 2u : OOB;
  float xo = bf_lo(xo16);
  unsigned wa, wc;
  {
    const int ra = 16 * (tq & 1) + 4 * g4 + (tq >> 1), rc = ra + 2;
    wa = L_RING + (unsigned)ra * 128u + (unsigned)(((2 * c + (tp >> 1)) ^ (((ra >> 1) & 3) << 1)) * 16 + (tp & 1) * 8);
    wc = L_RING + (unsigned)rc * 128u + (unsigned)(((2 * c + (tp >> 1)) ^ (((rc >> 1) & 3) << 1)) * 16 + (tp & 1) * 8);
  }
  unsigned so_part = WS_PART + (unsigned)((kvh * 16 + r16) * 128 + c * 16) * 8u, so_g1 = WS_PART + (unsigned)(ko * 2048 + c * 16) * 8u;
  unsigned so_s2 = (is_wo ? WS_SP + (unsigned)(cb * 16 + ko) * 1024u : WS_YP + (unsigned)(cbd * 12 + rbd) * 1024u) + (unsigned)c * 128u;
  unsigned so_fs = WS_SP + (unsigned)cb * 16384u, so_ft = so_fs + 8192u, so_fy = WS_YP + (unsigned)cb * 12288u;
  const rsrc_t rout = mk_rsrc(a.out, 2 * D * 2);
  asm volatile("" : "+v"(st_off), "+v"(g1_off), "+v"(s2_off), "+v"(fin_off), "+v"(fin_of2), "+v"(pa_w), "+v"(pa_w2), "+v"(pa_r), "+v"(o_off),
                    "+v"(xo), "+v"(wa), "+v"(wc));
  unsigned so_g2 = so_g1 + 8192u;
  asm volatile("" : "+s"(so_part), "+s"(so_g1), "+s"(so_g2), "+s"(so_s2), "+s"(so_fs), "+s"(so_ft), "+s"(so_fy));
  {
    const unsigned vs = (unsigned)((tq | ((g4 & 1) << 2)) << 1);
    const unsigned va0 = L_RING + (unsigned)(8 * g4 + tq) * 256u + (((unsigned)(4 * c + (tp >> 1)) ^ vs) * 16u) + (unsigned)(tp & 1) * 8u;
    const unsigned va1 = L_RING + (unsigned)(8 * g4 + tq) * 256u + (((unsigned)(4 * c + 2 + (tp >> 1)) ^ vs) * 16u) + (unsigned)(tp & 1) * 8u;
    const unsigned pad = L_P + (unsigned)(lane & 1) * P_STRIDE + (unsigned)g4 * 16u;
#pragma unroll 1
    for (int un = 0; un < 128; ++un) {
      bf16x8 pf = lds_frag(L, pad + un * 64);
      if (un == 120) {
        lds_wait_ge<1>(L, L_FLAGF, 1, budget); fnorm = *(LAS float*)(L + L_F + (g4 & 1) * 4);
      }
      if (landed < NQP + 1024 + 8 * (un + 1)) landed = lds_wait_ge<1>(L, L_LANDED, NQP + 1024 + 8 * (un + 1), budget);
      const unsigned ub = ((unsigned)un & 15u) * 8192u;
      bf16x8 b0 = lds_tr2(L, va0 + ub, va0 + ub + 1024), b1 = lds_tr2(L, va1 + ub, va1 + ub + 1024);
      asm volatile(LDS_PUT1 : "+v"(b0), "+v"(b1), "+v"(pf) : [pa] "v"(done_addr), [pv] "v"(S0 + 64 + ((un + 1) >> 1)) : "memory");
      o0 = mfma(pf, b0, o0);
      o1 = mfma(pf, b1, o1);
    }
  }
  STAMP(5);
  {
    const u32x2 gr = {pk_bf16(((g4 & 1) ? o0[1] : o0[0]) * fnorm, ((g4 & 1) ? o1[1] : o1[0]) * fnorm), tag};
    __builtin_amdgcn_raw_buffer_store_b64(gr, rws, st_off, so_part, AUX_SC1);
  }
  STAMP(6);
  if (is_wo) {
    const int tpoll = __builtin_amdgcn_readfirstlane(*(volatile LAS int*)(L + L_TEND)) + 72 * T_TK + 64 * T_TV + T_C1;
    pause(T_D1);
    wait_until(tpoll);
    u32x4 pv[4];
#pragma unroll
    for (int j = 0; j < 4; ++j) pv[j] = __builtin_amdgcn_raw_buffer_load_b128(rws, g1_off + (j >> 1) * 512, (j & 1) ? so_g2 : so_g1, AUX_SC1);
    lds_wait_ge<0>(L, L_LANDED, NQP + 2048 + 32, budget);
    bf16x8 wf[8];
#pragma unroll
    for (int ks = 0; ks < 8; ++ks) { wf[ks] = lds_tr2(L, wa + ks * 4096, wc + ks * 4096); }
    CFENCE();
    STAMP(7);
    unsigned long long open = ~0ull;
#pragma unroll 1
    for (;;) {
      asm volatile("v_cmpx_eq_u32 vcc, %[tag], %[y0]\n\tv_cmpx_eq_u32 vcc, %[tag], %[w0]\n\tv_cmpx_eq_u32 vcc, %[tag], %[y1]\n\t"
                   "v_cmpx_eq_u32 vcc, %[tag], %[w1]\n\tv_cmpx_eq_u32 vcc, %[tag], %[y2]\n\tv_cmpx_eq_u32 vcc, %[tag], %[w2]\n\t"
                   "v_cmpx_eq_u32 vcc, %[tag], %[y3]\n\tv_cmpx_eq_u32 vcc, %[tag], %[w3]\n\t"
                   "v_mov_b32 %[go], 2.0\n\t"
                   "ds_write2_b32 %[a0], %[x0], %[z0] offset1:1\n\tds_write2_b32 %[a0], %[x1], %[z1] offset0:160 offset1:161\n\t"
                   "ds_write2_b32 %[a1], %[x2], %[z2] offset1:1\n\tds_write2_b32 %[a1], %[x3], %[z3] offset0:160 offset1:161\n\t"
                   "s_andn2_b64 %[op], %[op], exec\n\ts_mov_b64 exec, -1"
                   : [go] "+v"(g1_off), [op] "+s"(open)
                   : [tag] "s"(tag), [a0] "v"(pa_w), [a1] "v"(pa_w2), [x0] "v"(pv[0].x), [y0] "v"(pv[0].y), [z0] "v"(pv[0].z), [w0] "v"(pv[0].w),
                     [x1] "v"(pv[1].x), [y1] "v"(pv[1].y), [z1] "v"(pv[1].z), [w1] "v"(pv[1].w), [x2] "v"(pv[2].x), [y2] "v"(pv[2].y),
                     [z2] "v"(pv[2].z), [w2] "v"(pv[2].w), [x3] "v"(pv[3].x), [y3] "v"(pv[3].y), [z3] "v"(pv[3].z), [w3] "v"(pv[3].w)
                   : "vcc", "scc", "memory");
      if (__builtin_expect(open == 0, 1) || (budget -= 64) < 0) break;
#pragma unroll
      for (int j = 0; j < 4; ++j) pv[j] = __builtin_amdgcn_raw_buffer_load_b128(rws, g1_off + (j >> 1) * 512, (j & 1) ? so_g2 : so_g1, AUX_SC1);
    }
    static_assert(OOB == 0x40000000u && 8 * PA_ROW == 640, "2.0f, offset 160");
    LGKM0();
    BARRIER();
    STAMP(8);
    {
      bf16x8 af[8];
#pragma unroll
      for (int ks = 0; ks < 8; ++ks) af[ks] = lds_frag(L, pa_r + ks * PA_KS);
      __builtin_amdgcn_sched_barrier(0);
      f32x4 d0 = {0.f, 0.f, 0.f, 0.f};
#pragma unroll
      for (int ks = 0; ks < 8; ++ks) d0 = mfma(af[ks], wf[ks], d0);
      const u32x2 gr = {__float_as_uint(x16_sum((d0[0] + d0[2]) + (d0[1] + d0[3]))), tag};
      __builtin_amdgcn_raw_buffer_store_b64(gr, rws, s2_off, so_s2, AUX_SC1);
    }
    STAMP(9);
    if (c == 2) {
      pause(T_D2);
      wait_until(tpoll + T_D3 * 256);
      u32x2 s0, s1, y0, y1;
#pragma unroll 1
      for (;;) {
        CFENCE();
        y0 = __builtin_amdgcn_raw_buffer_load_b64(rws, fin_off, so_fy, AUX_SC1);
        y1 = __builtin_amdgcn_raw_buffer_load_b64(rws, fin_of2, so_fy, AUX_SC1);
        s0 = __builtin_amdgcn_raw_buffer_load_b64(rws, fin_off, so_fs, AUX_SC1);
        s1 = __builtin_amdgcn_raw_buffer_load_b64(rws, fin_off, so_ft, AUX_SC1);
        if (__all((y0.y == tag) & (y1.y == tag) & (s0.y == tag) & (s1.y == tag)) || (budget -= 64) < 0) break;
      }
      STAMP(11);
      float y = __uint_as_float(y0.x) + (lane < 32 ? __uint_as_float(y1.x) : 0.f), sa = __uint_as_float(s0.x) + __uint_as_float(s1.x);
      y += dpp<DPP_ROR8>(y); sa += dpp<DPP_ROR8>(sa);
      auto e = __builtin_amdgcn_permlane16_swap(__float_as_uint(y), __float_as_uint(sa), false, false);
      const float t = rbf(x32_sum(__uint_as_float(e[0]) + __uint_as_float(e[1])));
      auto f = __builtin_amdgcn_permlane16_swap(__float_as_uint(t), __float_as_uint(t), false, false);
      const float r = xo + rbf(__uint_as_float(f[1]) * rbf(silu_f(t)));
      __builtin_amdgcn_raw_buffer_store_b16((unsigned short)pk_bf16(r, 0.f), rout, o_off, out_off, P_OAUX);
      if (wg == 0) __builtin_amdgcn_raw_buffer_store_b32(tag, rws, lane == 0 ? 0u : OOB, WS_EPOCH, AUX_SC1);
      STAMP(10);
    }
  } else if (is_wd) {
    lds_wait_ge<1>(L, L_FLAGH, 1, budget);
    f32x4 d = {0.f, 0.f, 0.f, 0.f};
#pragma unroll 1
    for (int k2 = 0; k2 < 6; ++k2) {
      const bf16x8 h0 = lds_frag(L, L_HB + (lane & 1) * HB_STRIDE + k2 * 128 + g4 * 16), h1 = lds_frag(L, L_HB + (lane & 1) * HB_STRIDE + k2 * 128 + 64 + g4 * 16);
      if (landed < NQP + 2048 + 8 * (k2 + 1)) landed = lds_wait_ge<0>(L, L_LANDED, NQP + 2048 + 8 * (k2 + 1), budget);
      d = mfma(h0, lds_tr2(L, L_RING + k2 * 8192 + wt0, L_RING + k2 * 8192 + wt1), d);
      d = mfma(h1, lds_tr2(L, L_RING + k2 * 8192 + 4096 + wt0, L_RING + k2 * 8192 + 4096 + wt1), d);
    }
    {
      const u32x2 gr = {__float_as_uint(g4 ? d[1] : d[0]), tag};
      __builtin_amdgcn_raw_buffer_store_b64(gr, rws, st_off, so_s2, AUX_SC1);
    }
    STAMP(12);
  }
  STAMP_DUMP();
}

}

extern "C" void kernel_launch(void* const* d_in, const int* in_sizes, int n_in, void* d_out, int out_size, void* d_ws,
                              size_t ws_size, hipStream_t stream) {
  Args a;
  a.x = d_in[0]; a.k = d_in[1]; a.v = d_in[2]; a.wq = d_in[3]; a.wo = d_in[4];
  a.wg = d_in[6]; a.wu = d_in[7]; a.wd = d_in[8]; a.out = d_out; a.ws = d_ws;
  hipLaunchKernelGGL(fused_decode, dim3(NWG), dim3(NTHR), 0, stream, a);
}
```
